```python
import torch

def _softmax(x, axis=-1):
    return torch.softmax(x, dim=axis)
def _gelu(x):
    return torch.nn.functional.gelu(x, approximate="tanh")
def _silu(x):
    return torch.nn.functional.silu(x)
def _sigmoid(x):
    return torch.sigmoid(x)
def _layernorm(x, axis=-1, eps=1e-5):
    m = x.mean(dim=axis, keepdim=True)
    v = x.var(dim=axis, keepdim=True, unbiased=False)
    return (x - m) / torch.sqrt(v + eps)
def _scatter_add(tgt, idx, src, axis=0):
    return tgt.clone().index_add_(axis, idx, src)
def _segment_sum(src, seg, n):
    out = torch.zeros((n,) + src.shape[1:], dtype=src.dtype)
    return out.index_add_(0, seg, src)
def _pad(x, pw):
    flat = tuple(v for pair in reversed(pw) for v in pair)
    return torch.nn.functional.pad(x, flat)

kernel_name = 'sequential_preLN__ssm_conv_h3__glu_gated_reglu__prompt'

def setup_inputs():

    _w = (lambda a: (lambda t: t.to(torch.bfloat16) if t.is_floating_point() else t)(torch.as_tensor(a)))
    def _A_setup():
        B, S, D = 1, 2048, 640
        ks = 4
        return {
            "q": _w((lambda *s: torch.randn(*s, dtype=torch.bfloat16) * 0.05)(B, S, D)),
            "k": _w((lambda *s: torch.randn(*s, dtype=torch.bfloat16) * 0.2)(B, S, D)),
            "v": _w((lambda *s: torch.randn(*s, dtype=torch.bfloat16) * 0.2)(B, S, D)),
            "shift_k": _w((lambda *s: torch.randn(*s, dtype=torch.bfloat16) * 0.2)(D, ks) * 0.5),
            "ssm_k": _w((lambda *s: torch.randn(*s, dtype=torch.bfloat16) * 0.4)(D, S) * (1.0 / S ** 0.5)),
            "ssm_D": _w((lambda *s: torch.randn(*s, dtype=torch.bfloat16) * 0.2)(D)),
        }
    _A_in = _A_setup()

    def _B_setup():
        B, S, D, F = 1, 2048, 640, 1536
        return {
            "x": _w((lambda *s: torch.randn(*s, dtype=torch.bfloat16) * 0.05)(B, S, D)),
            "W_gate": _w((lambda *s: torch.randn(*s, dtype=torch.bfloat16) * 0.2)(D, F) * (2.0 / D) ** 0.5),
            "W_up": _w((lambda *s: torch.randn(*s, dtype=torch.bfloat16) * 0.2)(D, F) * (2.0 / D) ** 0.5),
            "W_down": _w((lambda *s: torch.randn(*s, dtype=torch.bfloat16) * 0.2)(F, D) * (1.0 / F) ** 0.5),
        }
    _B_in = _B_setup()

    return {
        "A_q": _A_in["q"],
        "A_k": _A_in["k"],
        "A_v": _A_in["v"],
        "A_shift_k": _A_in["shift_k"],
        "A_ssm_k": _A_in["ssm_k"],
        "A_ssm_D": _A_in["ssm_D"],
        "B_x": _B_in["x"],
        "B_W_gate": _B_in["W_gate"],
        "B_W_up": _B_in["W_up"],
        "B_W_down": _B_in["W_down"],
    }

def reference(A_q, A_k, A_v, A_shift_k, A_ssm_k, A_ssm_D, B_x, B_W_gate, B_W_up, B_W_down):
    def _A_ref(q, k, v, shift_k, ssm_k, ssm_D):
        B, S, D = q.shape
        ks = shift_k.shape[-1]
        k_pad = _pad(k, ((0, 0), (ks - 1, 0), (0, 0)))
        k_shift = torch.zeros_like(k)
        for t in range(ks):
            k_shift = k_shift + k_pad[:, t:t + S, :] * shift_k[:, ks - 1 - t][None, None, :]
        kv = k_shift * v
        L = 2 * S
        kv_pad = _pad(kv, ((0, 0), (0, S), (0, 0)))
        kf_pad = _pad(ssm_k, ((0, 0), (0, S)))
        kv_f = (lambda x, axes=(-2,-1): torch.fft.rfft2(x.float(), dim=axes))(kv_pad[:, None, :, :], axes=(1, 2))
        kf_f = (lambda x, axes=(-2,-1): torch.fft.rfft2(x.float(), dim=axes))(kf_pad[:, None, :], axes=(1, 2))
        kf_f = (lambda x, *d: x.permute(*d))(kf_f, 1, 2, 0)
        y_f = kv_f * kf_f[None, :, :, :]
        y = (lambda x, s, axes=(-2,-1): torch.fft.irfft2(x, s=s, dim=axes).to(torch.bfloat16))(y_f, (1, L), axes=(1, 2))[:, 0, :S, :]
        y = y + kv * ssm_D[None, None, :]
        return q * y

    def _B_ref(x, W_gate, W_up, W_down):
        gate = torch.einsum("bsd,df->bsf", x, W_gate)
        up = torch.einsum("bsd,df->bsf", x, W_up)
        gate = gate * (lambda x, dt: x.to(dt))(gate > 0, gate.dtype)
        h = gate * up
        return torch.einsum("bsf,fd->bsd", h, W_down)

    x = A_q
    h = x + _A_ref(_layernorm(x, axis=-1), A_k, A_v, A_shift_k, A_ssm_k, A_ssm_D)
    h2 = h + _B_ref(_layernorm(h, axis=-1), B_W_gate, B_W_up, B_W_down)
    return h2
```

```cpp
#include <hip/hip_runtime.h>
#include <hip/hip_bf16.h>
#include <stdint.h>

typedef unsigned short u16;
#define S_ 2048
#define D_ 640
#define F_ 1536

__device__ __forceinline__ float bf2f(u16 x) { return __uint_as_float(((unsigned)x) << 16); }
__device__ __forceinline__ u16 f2bf(float f) { __bf16 b = (__bf16)f; return __builtin_bit_cast(u16, b); }
__device__ __forceinline__ float rbf(float f) { return bf2f(f2bf(f)); }

__global__ void k_kv(const u16* k, const u16* v, const u16* sk, u16* kv) {
    int i = blockIdx.x * 256 + threadIdx.x;
    int t = i / D_, d = i % D_;
    float acc = 0.f;
    for (int j = 0; j < 4; ++j) {
        int tt = t - 3 + j;
        float kk = tt >= 0 ? bf2f(k[tt * D_ + d]) : 0.f;
        acc = rbf(acc + rbf(kk * bf2f(sk[d * 4 + 3 - j])));
    }
    kv[i] = f2bf(acc * bf2f(v[i]));
}
__global__ void k_ln(const u16* x, u16* o) {
    int t = blockIdx.x;
    __shared__ float red[2][64];
    float s = 0.f;
    for (int d = threadIdx.x; d < D_; d += 64) s += bf2f(x[t * D_ + d]);
    for (int off = 32; off; off >>= 1) s += __shfl_xor(s, off);
    float mean = s / D_;
    float q = 0.f;
    for (int d = threadIdx.x; d < D_; d += 64) { float z = bf2f(x[t * D_ + d]) - mean; q += z * z; }
    for (int off = 32; off; off >>= 1) q += __shfl_xor(q, off);
    float m = rbf(mean), var = rbf(q / D_);
    float sd = rbf(sqrtf(rbf(var + 1e-5f)));
    for (int d = threadIdx.x; d < D_; d += 64) o[t * D_ + d] = f2bf(rbf(bf2f(x[t * D_ + d]) - m) / sd);
}
__global__ void k_conv(const u16* kv, const u16* h, const u16* Dv, const u16* x, const u16* q, u16* out) {
    int i = blockIdx.x * 256 + threadIdx.x;
    int t = i / D_, d = i % D_;
    float acc = 0.f;
    for (int s = 0; s <= t; ++s) acc += bf2f(kv[s * D_ + d]) * bf2f(h[d * S_ + t - s]);
    float y = rbf(acc);
    y = rbf(y + rbf(bf2f(kv[i]) * bf2f(Dv[d])));
    out[i] = f2bf(bf2f(x[i]) + rbf(bf2f(q[i]) * y));
}
__global__ void k_g1(const u16* a, const u16* wg, const u16* wu, u16* H) {
    int i = blockIdx.x * 256 + threadIdx.x;
    int t = i / F_, f = i % F_;
    float g = 0.f, u = 0.f;
    for (int d = 0; d < D_; ++d) { float z = bf2f(a[t * D_ + d]); g += z * bf2f(wg[d * F_ + f]); u += z * bf2f(wu[d * F_ + f]); }
    g = rbf(g); u = rbf(u);
    g = g > 0.f ? g : 0.f;
    H[i] = f2bf(g * u);
}
__global__ void k_g2(const u16* H, const u16* wd, const u16* h, u16* out) {
    int i = blockIdx.x * 256 + threadIdx.x;
    int t = i / D_, d = i % D_;
    float a = 0.f;
    for (int f = 0; f < F_; ++f) a += bf2f(H[t * F_ + f]) * bf2f(wd[f * D_ + d]);
    out[i] = f2bf(bf2f(h[i]) + rbf(a));
}

extern "C" void kernel_launch(void* const* d_in, const int* in_sizes, int n_in,
                              void* d_out, int out_size, void* d_ws, size_t ws_size,
                              hipStream_t stream) {
    const u16* q = (const u16*)d_in[0];
    const u16* k = (const u16*)d_in[1];
    const u16* v = (const u16*)d_in[2];
    const u16* sk = (const u16*)d_in[3];
    const u16* hk = (const u16*)d_in[4];
    const u16* Dv = (const u16*)d_in[5];
    const u16* wg = (const u16*)d_in[7];
    const u16* wu = (const u16*)d_in[8];
    const u16* wd = (const u16*)d_in[9];
    u16* ws = (u16*)d_ws;
    u16* kv = ws; u16* ln1 = ws + S_ * D_; u16* h = ws + 2 * S_ * D_; u16* ln2 = ws + 3 * S_ * D_; u16* H = ws + 4 * S_ * D_;
    k_kv<<<S_ * D_ / 256, 256, 0, stream>>>(k, v, sk, kv);
    k_ln<<<S_, 64, 0, stream>>>(q, ln1);
    k_conv<<<S_ * D_ / 256, 256, 0, stream>>>(kv, hk, Dv, q, ln1, h);
    k_ln<<<S_, 64, 0, stream>>>(h, ln2);
    k_g1<<<S_ * F_ / 256, 256, 0, stream>>>(ln2, wg, wu, H);
    k_g2<<<S_ * D_ / 256, 256, 0, stream>>>(H, wd, h, (u16*)d_out);
}
```

```cpp
#include <hip/hip_runtime.h>
#include <hip/hip_bf16.h>
#include <stdint.h>

typedef unsigned short u16;
typedef unsigned int u32;
typedef __attribute__((ext_vector_type(2))) unsigned int u32x2;
typedef __attribute__((ext_vector_type(4))) unsigned int u32x4;
typedef __attribute__((ext_vector_type(4))) float f32x4;
typedef __attribute__((ext_vector_type(2))) float f32x2;
typedef __attribute__((ext_vector_type(8))) __bf16 bf16x8;
typedef __attribute__((ext_vector_type(4))) short s16x4;
typedef __attribute__((ext_vector_type(2))) __bf16 bf16x2;
#define LDS_AS __attribute__((address_space(3)))

#define S_ 2048
#define D_ 640
#define F_ 1536

#define WS_UR   0
#define WS_XT   (WS_UR + D_ * S_ * 2)
#define WS_ST1  (WS_XT + D_ * S_ * 2)
#define WS_HT   (WS_ST1 + S_ * 8)
#define WS_WGU  (WS_HT + D_ * S_ * 2)
#define WS_CS   (WS_WGU + D_ * F_ * 2 * 2)
#define WS_WD   (WS_CS + 5 * 3072 * 4)
#define WS_HF   (WS_WD + D_ * F_ * 2)
#define WS_END  (WS_HF + S_ * F_ * 2)

__device__ __forceinline__ float bflo(u32 x) { return __uint_as_float(x << 16); }
__device__ __forceinline__ float bfhi(u32 x) { return __uint_as_float(x & 0xffff0000u); }
__device__ __forceinline__ u32 pk2(float lo, float hi) {
    bf16x2 r; r[0] = (__bf16)lo; r[1] = (__bf16)hi;
    return __builtin_bit_cast(u32, r);
}
__device__ __forceinline__ f32x4 mfma16(u32x4 a, u32x4 b, f32x4 c) {
    return __builtin_amdgcn_mfma_f32_16x16x32_bf16(__builtin_bit_cast(bf16x8, a), __builtin_bit_cast(bf16x8, b), c, 0, 0, 0);
}
__device__ __forceinline__ float dot2(u32 a, u32 b, float c) {
    return __builtin_amdgcn_fdot2_f32_bf16(__builtin_bit_cast(bf16x2, a), __builtin_bit_cast(bf16x2, b), c, false);
}
__device__ __forceinline__ void glds16(const void* gsrc, u32 lds_dst) {
    u32 keep;
    asm volatile("s_mov_b32 %0, m0\n\ts_mov_b32 m0, %2\n\ts_nop 0\n\tglobal_load_lds_dwordx4 %1, off\n\ts_mov_b32 m0, %0"
                 : "=&s"(keep) : "v"(gsrc), "s"(lds_dst) : "memory");
}
#define WAIT_VM(n) asm volatile("s_waitcnt vmcnt(" #n ")" ::: "memory")
template <int N> __device__ __forceinline__ void wait_vm() { asm volatile("s_waitcnt vmcnt(%0)" :: "i"(N) : "memory"); }

__device__ __forceinline__ float wave_sum(float v) {
#pragma unroll
    for (int o = 32; o; o >>= 1) v += __shfl_xor(v, o);
    return v;
}

__global__ __launch_bounds__(256) void k1_prep(const u16* __restrict__ x, const u16* __restrict__ k, const u16* __restrict__ v,
                                               const u16* __restrict__ sk, u16* __restrict__ uR, u16* __restrict__ xT, f32x2* __restrict__ st1) {
    const int b = blockIdx.x, tid = threadIdx.x;
    if (b < 320) {
        const int tb = b / 10, cb = b % 10;
        const int cp = tid & 31, tg = tid >> 5;
        const int c0 = cb * 64 + cp * 2, t0 = tb * 64 + tg * 8;
        u32 kk[11], vv[8], xx[8];
#pragma unroll
        for (int j = 0; j < 11; ++j) {
            int t = t0 - 3 + j;
            kk[j] = *(const u32*)(k + (size_t)(t < 0 ? 0 : t) * D_ + c0);
        }
#pragma unroll
        for (int j = 0; j < 8; ++j) vv[j] = *(const u32*)(v + (size_t)(t0 + j) * D_ + c0);
#pragma unroll
        for (int j = 0; j < 8; ++j) xx[j] = *(const u32*)(x + (size_t)(t0 + j) * D_ + c0);
        u32x4 w = *(const u32x4*)(sk + c0 * 4);
        if (t0 == 0) { kk[0] = 0; kk[1] = 0; kk[2] = 0; }
        float wa[4] = {bflo(w[0]), bfhi(w[0]), bflo(w[1]), bfhi(w[1])};
        float wb[4] = {bflo(w[2]), bfhi(w[2]), bflo(w[3]), bfhi(w[3])};
        float ua[8], ub[8];
#pragma unroll
        for (int j = 0; j < 8; ++j) {
            float a = bflo(kk[j]) * wa[3], c = bfhi(kk[j]) * wb[3];
            a = fmaf(bflo(kk[j + 1]), wa[2], a); c = fmaf(bfhi(kk[j + 1]), wb[2], c);
            a = fmaf(bflo(kk[j + 2]), wa[1], a); c = fmaf(bfhi(kk[j + 2]), wb[1], c);
            a = fmaf(bflo(kk[j + 3]), wa[0], a); c = fmaf(bfhi(kk[j + 3]), wb[0], c);
            ua[j] = a * bflo(vv[j]); ub[j] = c * bfhi(vv[j]);
        }
        u32x4 oa, ob, xa, xb;
#pragma unroll
        for (int j = 0; j < 4; ++j) {
            oa[j] = pk2(ua[7 - 2 * j], ua[6 - 2 * j]);
            ob[j] = pk2(ub[7 - 2 * j], ub[6 - 2 * j]);
            xa[j] = __builtin_amdgcn_perm(xx[2 * j + 1], xx[2 * j], 0x05040100u);
            xb[j] = __builtin_amdgcn_perm(xx[2 * j + 1], xx[2 * j], 0x07060302u);
        }
        *(u32x4*)(uR + (size_t)c0 * S_ + (S_ - 8 - t0)) = oa;
        *(u32x4*)(uR + (size_t)(c0 + 1) * S_ + (S_ - 8 - t0)) = ob;
        *(u32x4*)(xT + (size_t)c0 * S_ + t0) = xa;
        *(u32x4*)(xT + (size_t)(c0 + 1) * S_ + t0) = xb;
    } else {
        const int lane = tid & 63, w = tid >> 6;
        const int t0 = (b - 320) * 8 + w * 2;
        u32 r[2][5];
#pragma unroll
        for (int i = 0; i < 2; ++i)
#pragma unroll
            for (int j = 0; j < 5; ++j) r[i][j] = *(const u32*)(x + (size_t)(t0 + i) * D_ + (j * 64 + lane) * 2);
#pragma unroll
        for (int i = 0; i < 2; ++i) {
            float p = bflo(__builtin_amdgcn_readfirstlane(r[i][0]));
            float s = 0.f, q = 0.f;
#pragma unroll
            for (int j = 0; j < 5; ++j) {
                float a = bflo(r[i][j]) - p, c = bfhi(r[i][j]) - p;
                s += a + c; q = fmaf(a, a, q); q = fmaf(c, c, q);
            }
            s = wave_sum(s); q = wave_sum(q);
            float dm = s * (1.f / D_);
            float var = fmaxf(q * (1.f / D_) - dm * dm, 0.f);
            float rs = rsqrtf(var + 1e-5f);
            if (lane == 0) { f32x2 o; o[0] = rs; o[1] = -(p + dm) * rs; st1[t0 + i] = o; }
        }
    }
}

#define K2_CPY 4160
#define K2_U   16640
#define K2_X   21248
#define K2_WV  25344
#define K2_ST  (4 * K2_WV)
#define K2_LDS (K2_ST + 16384)

__global__ __launch_bounds__(256, 1) void k2_conv(const u16* __restrict__ hk, const u16* __restrict__ Dv,
                                                  const u16* __restrict__ uR, const u16* __restrict__ xT, const f32x2* __restrict__ st1,
                                                  u16* __restrict__ hT,
                                                  const u16* __restrict__ wg, const u16* __restrict__ wu, const u16* __restrict__ wd,
                                                  u16* __restrict__ wgu_t, float* __restrict__ cs, u16* __restrict__ wd_t) {
    extern __shared__ __attribute__((aligned(16))) char smem[];
    const int b = blockIdx.x, tid = threadIdx.x, lane = tid & 63;
    const int w = __builtin_amdgcn_readfirstlane(tid >> 6);
    if (b < 160) {
        const int d = b * 4 + w;
        const u32 lb = (u32)(size_t)(LDS_AS char*)smem;
        const u32 wb = lb + w * K2_WV;
        const u16* hrow = hk + (size_t)d * S_;
        {
            const u16* ur = uR + (size_t)d * S_ + lane * 8;
#pragma unroll
            for (int kb = 0; kb < 4; ++kb) glds16(ur + kb * 512, wb + K2_U + kb * 1024);
            const u16* hend = hk + (size_t)D_ * S_ - 8;
#pragma unroll
            for (int kb = 0; kb < 4; ++kb)
#pragma unroll
                for (int c = 0; c < 4; ++c) {
                    const u16* p = hrow + kb * 512 + lane * 8 + c;
                    if (kb == 3 && c > 0) p = p > hend ? hend : p;
                    glds16(p, wb + c * K2_CPY + 32 + kb * 1024);
                }
            const u16* sp = (const u16*)st1 + w * 2048 + lane * 8;
#pragma unroll
            for (int kb = 0; kb < 4; ++kb) glds16(sp + kb * 512, lb + K2_ST + w * 4096 + kb * 1024);
            const u16* xr = xT + (size_t)d * S_ + lane * 8;
#pragma unroll
            for (int kb = 0; kb < 4; ++kb) glds16(xr + kb * 512, wb + K2_X + kb * 1024);
        }
        LDS_AS char* wl = (LDS_AS char*)smem + w * K2_WV;
        {
            u32x2 z = {0u, 0u};
            *(LDS_AS u32x2*)(wl + K2_U + 4096 + lane * 8) = z;
            u32x2 h01 = *(const u32x2*)hrow;
            const int c = lane & 3, m = (lane >> 2) & 1;
            u32 d2 = 0u, d3 = 0u;
            if (m == 1) {
                if (c == 1) d3 = h01[0] << 16;
                if (c == 2) d3 = h01[0];
                if (c == 3) { d2 = h01[0] << 16; d3 = (h01[0] >> 16) | (h01[1] << 16); }
            }
            u32x4 ch = {0u, 0u, d2, d3};
            if (lane < 8) *(LDS_AS u32x4*)(wl + c * K2_CPY + m * 16) = ch;
        }
        const int a = lane & 15, g = lane >> 4;
        u32x4 AD;
        {
            u32 dv = Dv[d];
            int jj = 15 - a - 8 * g;
#pragma unroll
            for (int i = 0; i < 4; ++i) AD[i] = (jj == 2 * i) ? dv : ((jj == 2 * i + 1) ? (dv << 16) : 0u);
        }
        const int o = a + 1 + 8 * g, cc = o & 3;
        LDS_AS char* pA0 = wl + cc * K2_CPY + (o - cc) * 2;
        LDS_AS char* pA1 = pA0 + 8;
        asm volatile("" : "+v"(pA1));
        LDS_AS char* pB = wl + K2_U + (2032 - 16 * a + 8 * g) * 2 - 56 * 64;
        f32x4 acc[8];
#pragma unroll
        for (int i = 0; i < 8; ++i) acc[i] = (f32x4){0.f, 0.f, 0.f, 0.f};
        u32x4 B[8][8];
        wait_vm<20>();
#pragma unroll
        for (int q = 0; q < 8; ++q) {
            if (q == 2) wait_vm<16>();
            if (q == 4) wait_vm<12>();
            if (q == 6) wait_vm<8>();
#pragma unroll
            for (int e0 = 0; e0 < 8; ++e0) {
                const int e = 8 * q + e0;
                u32x2 a0 = *(LDS_AS u32x2*)(pA0 + 64 * e);
                u32x2 a1 = *(LDS_AS u32x2*)(pA1 + 64 * e);
                u32x4 A = {a0[0], a0[1], a1[0], a1[1]};
                if (q == 0) {
#pragma unroll
                    for (int p = 0; p < 8; ++p) B[p][e0] = *(LDS_AS u32x4*)(pB + (56 - (8 * p - e0)) * 64);
                }
#pragma unroll
                for (int p = 0; p + q < 8; ++p) acc[p + q] = mfma16(A, B[p][e0], acc[p + q]);
                if (q == 0 && e0 == 0) {
#pragma unroll
                    for (int p = 0; p < 8; ++p) acc[p] = mfma16(AD, B[p][0], acc[p]);
                }
            }
        }
        wait_vm<0>();
        __builtin_amdgcn_s_barrier();
        LDS_AS char* pS = (LDS_AS char*)smem + K2_ST + (16 * a + 4 * g) * 8;
        LDS_AS char* pX = wl + K2_X + (16 * a + 4 * g) * 2;
        u16* op = hT + ((size_t)(((a >> 3) * 20 + (d >> 5)) * 8 + (a & 7)) * 32 + (d & 31)) * 16 + 4 * g;
#pragma unroll
        for (int i = 0; i < 8; ++i) {
            f32x4 s0 = *(LDS_AS f32x4*)(pS + i * 2048);
            f32x4 s1 = *(LDS_AS f32x4*)(pS + i * 2048 + 16);
            u32x2 xv = *(LDS_AS u32x2*)(pX + i * 512);
            float x0 = bflo(xv[0]), x1 = bfhi(xv[0]), x2 = bflo(xv[1]), x3 = bfhi(xv[1]);
            float h0 = fmaf(fmaf(x0, s0[0], s0[1]), acc[i][0], x0);
            float h1 = fmaf(fmaf(x1, s0[2], s0[3]), acc[i][1], x1);
            float h2 = fmaf(fmaf(x2, s1[0], s1[1]), acc[i][2], x2);
            float h3 = fmaf(fmaf(x3, s1[2], s1[3]), acc[i][3], x3);
            u32x2 ov = {pk2(h0, h1), pk2(h2, h3)};
            *(u32x2*)(op + (size_t)i * (2 * 20 * 8 * 512)) = ov;
        }
    } else if (b < 220) {
        const int bb = b - 160, ksg = bb / 12, cpb = bb % 12;
        const int g = w, col = (cpb * 64 + lane) * 2;
        const int nb = col / 96, rem = col % 96, wv = rem / 24, tile = (rem % 24) / 8, gp = (rem % 8) / 2;
        u32 G[4][8], U[4][8];
#pragma unroll
        for (int s = 0; s < 4; ++s)
#pragma unroll
            for (int j = 0; j < 8; ++j) {
                const int kr = (ksg * 4 + s) * 32 + 16 * (j >> 2) + 4 * g + (j & 3);
                G[s][j] = *(const u32*)(wg + (size_t)kr * F_ + col);
                U[s][j] = *(const u32*)(wu + (size_t)kr * F_ + col);
            }
        float sm[4] = {0.f, 0.f, 0.f, 0.f};
        const u32 ones = 0x3f803f80u;
#pragma unroll
        for (int s = 0; s < 4; ++s) {
            u32x4 r0, r1, r2, r3;
#pragma unroll
            for (int j = 0; j < 4; ++j) {
                r0[j] = __builtin_amdgcn_perm(G[s][2 * j + 1], G[s][2 * j], 0x05040100u);
                r1[j] = __builtin_amdgcn_perm(G[s][2 * j + 1], G[s][2 * j], 0x07060302u);
                r2[j] = __builtin_amdgcn_perm(U[s][2 * j + 1], U[s][2 * j], 0x05040100u);
                r3[j] = __builtin_amdgcn_perm(U[s][2 * j + 1], U[s][2 * j], 0x07060302u);
                sm[0] = dot2(r0[j], ones, sm[0]); sm[1] = dot2(r1[j], ones, sm[1]);
                sm[2] = dot2(r2[j], ones, sm[2]); sm[3] = dot2(r3[j], ones, sm[3]);
            }
            u16* op = wgu_t + ((size_t)(((nb * 4 + wv) * 20 + ksg * 4 + s) * 3 + tile) * 64 + 16 * g + 4 * gp) * 8;
            *(u32x4*)(op) = r0; *(u32x4*)(op + 8) = r1; *(u32x4*)(op + 16) = r2; *(u32x4*)(op + 24) = r3;
        }
        LDS_AS f32x4* red = (LDS_AS f32x4*)smem;
        red[w * 64 + lane] = (f32x4){sm[0], sm[1], sm[2], sm[3]};
        __syncthreads();
        if (w == 0) {
            f32x4 t = red[lane] + red[64 + lane] + red[128 + lane] + red[192 + lane];
            *(f32x4*)(cs + ksg * 3072 + ((nb * 4 + wv) * 3 + tile) * 16 + 4 * gp) = t;
        }
    } else {
        const int bb = b - 220, sg = bb / 5, dpb = bb % 5;
        const int g = w, dc = (dpb * 64 + lane) * 2;
        const int db = dc / 80, dt = (dc % 80) / 16, dl = dc % 16;
        u32 Wv[8][8];
#pragma unroll
        for (int s = 0; s < 8; ++s)
#pragma unroll
            for (int j = 0; j < 8; ++j) {
                const int f = (sg * 8 + s) * 32 + 8 * (j >> 1) + 2 * g + (j & 1);
                Wv[s][j] = *(const u32*)(wd + (size_t)f * D_ + dc);
            }
#pragma unroll
        for (int s = 0; s < 8; ++s) {
            u32x4 r0, r1;
#pragma unroll
            for (int j = 0; j < 4; ++j) {
                r0[j] = __builtin_amdgcn_perm(Wv[s][2 * j + 1], Wv[s][2 * j], 0x05040100u);
                r1[j] = __builtin_amdgcn_perm(Wv[s][2 * j + 1], Wv[s][2 * j], 0x07060302u);
            }
            u16* op = wd_t + ((size_t)((db * 48 + sg * 8 + s) * 5 + dt) * 64 + 16 * g + dl) * 8;
            *(u32x4*)(op) = r0; *(u32x4*)(op + 8) = r1;
        }
    }
}

#define K3_NSLOT 12
#define K3_WIN 8
#define K3_MISC (K3_NSLOT * 8192)
#define K3_LDS (K3_MISC + 1024 + 768)

__device__ __forceinline__ u32x4 gload16(const void* p) {
    u32x4 v;
    asm volatile("global_load_dwordx4 %0, %1, off" : "=v"(v) : "v"(p) : "memory");
    return v;
}
__device__ __forceinline__ u32x2 trread(LDS_AS char* p) {
    s16x4 r = __builtin_amdgcn_ds_read_tr16_b64_v4i16((LDS_AS s16x4*)p);
    return __builtin_bit_cast(u32x2, r);
}

__global__ __launch_bounds__(256, 1) void k3_gemm1(const u16* __restrict__ hT, const u16* __restrict__ wgu_t, const float* __restrict__ cs,
                                                   u16* __restrict__ Hf) {
    extern __shared__ __attribute__((aligned(16))) char smem[];
    const int tid = threadIdx.x, lane = tid & 63;
    const int w = __builtin_amdgcn_readfirstlane(tid >> 6);
    const int xcd = blockIdx.x & 7, idx = blockIdx.x >> 3;
    const int tb = (xcd & 1) * 8 + (idx & 7), nb = (xcd >> 1) * 4 + (idx >> 3);
    const u32 lb = (u32)(size_t)(LDS_AS char*)smem;
    const u16* hsrc = hT + (size_t)tb * (20 * 4096) + w * 1024 + lane * 8;
    const u16* wsrc = wgu_t + (size_t)(nb * 4 + w) * (20 * 1536) + lane * 8;
    if (tid < 192) {
        float t = 0.f;
#pragma unroll
        for (int i = 0; i < 5; ++i) t += cs[i * 3072 + nb * 192 + tid];
        *(LDS_AS float*)((LDS_AS char*)smem + K3_MISC + 1024 + tid * 4) = t;
    }
    u32x4 W[20][3];
#define K3_ISSUE(s) do { \
        glds16(hsrc + (s) * 4096, lb + ((s) % K3_NSLOT) * 8192 + w * 2048); \
        glds16(hsrc + (s) * 4096 + 512, lb + ((s) % K3_NSLOT) * 8192 + w * 2048 + 1024); \
        W[s][0] = gload16(wsrc + (s) * 1536); W[s][1] = gload16(wsrc + (s) * 1536 + 512); W[s][2] = gload16(wsrc + (s) * 1536 + 1024); \
    } while (0)
#pragma unroll
    for (int s = 0; s < K3_WIN; ++s) K3_ISSUE(s);
    f32x4 acc[8][3];
#pragma unroll
    for (int i = 0; i < 8; ++i)
#pragma unroll
        for (int j = 0; j < 3; ++j) acc[i][j] = (f32x4){0.f, 0.f, 0.f, 0.f};
    float ssum[2] = {0.f, 0.f}, ssq[2] = {0.f, 0.f};
    const u32 ones = 0x3f803f80u;
    LDS_AS char* hp = (LDS_AS char*)smem + lane * 8;
#pragma unroll
    for (int s = 0; s < 20; ++s) {
        {
            constexpr int dummy = 0; (void)dummy;
            const int last = (s + K3_WIN - 1 < 19 ? s + K3_WIN - 1 : 19);
            switch (last - s) {
                case 7: asm volatile("s_waitcnt vmcnt(35)" : "+v"(W[s][0]), "+v"(W[s][1]), "+v"(W[s][2]) :: "memory"); break;
                case 6: asm volatile("s_waitcnt vmcnt(30)" : "+v"(W[s][0]), "+v"(W[s][1]), "+v"(W[s][2]) :: "memory"); break;
                case 5: asm volatile("s_waitcnt vmcnt(25)" : "+v"(W[s][0]), "+v"(W[s][1]), "+v"(W[s][2]) :: "memory"); break;
                case 4: asm volatile("s_waitcnt vmcnt(20)" : "+v"(W[s][0]), "+v"(W[s][1]), "+v"(W[s][2]) :: "memory"); break;
                case 3: asm volatile("s_waitcnt vmcnt(15)" : "+v"(W[s][0]), "+v"(W[s][1]), "+v"(W[s][2]) :: "memory"); break;
                case 2: asm volatile("s_waitcnt vmcnt(10)" : "+v"(W[s][0]), "+v"(W[s][1]), "+v"(W[s][2]) :: "memory"); break;
                case 1: asm volatile("s_waitcnt vmcnt(5)" : "+v"(W[s][0]), "+v"(W[s][1]), "+v"(W[s][2]) :: "memory"); break;
                default: asm volatile("s_waitcnt vmcnt(0)" : "+v"(W[s][0]), "+v"(W[s][1]), "+v"(W[s][2]) :: "memory"); break;
            }
        }
        asm volatile("s_waitcnt lgkmcnt(0)" ::: "memory");
        __builtin_amdgcn_s_barrier();
        asm volatile("" ::: "memory");
        if (s + K3_WIN < 20) K3_ISSUE(s + K3_WIN);
#pragma unroll
        for (int tt = 0; tt < 8; ++tt) {
            u32x2 h0 = trread(hp + (s % K3_NSLOT) * 8192 + tt * 1024);
            u32x2 h1 = trread(hp + (s % K3_NSLOT) * 8192 + tt * 1024 + 512);
            u32x4 hf = {h0[0], h0[1], h1[0], h1[1]};
#pragma unroll
            for (int j = 0; j < 3; ++j) acc[tt][j] = mfma16(W[s][j], hf, acc[tt][j]);
            if ((tt >> 1) == w) {
#pragma unroll
                for (int i = 0; i < 4; ++i) { ssum[tt & 1] = dot2(hf[i], ones, ssum[tt & 1]); ssq[tt & 1] = dot2(hf[i], hf[i], ssq[tt & 1]); }
            }
        }
    }
    LDS_AS f32x2* stl = (LDS_AS f32x2*)((LDS_AS char*)smem + K3_MISC);
#pragma unroll
    for (int i = 0; i < 2; ++i) {
        float s = ssum[i], q = ssq[i];
        s += __shfl_xor(s, 16); q += __shfl_xor(q, 16);
        s += __shfl_xor(s, 32); q += __shfl_xor(q, 32);
        float m = s * (1.f / D_);
        float var = fmaxf(q * (1.f / D_) - m * m, 0.f);
        float r2 = 1.f / (var + 1e-5f);
        if (lane < 16) stl[(2 * w + i) * 16 + lane] = (f32x2){m, r2};
    }
    __syncthreads();
    const int g = lane >> 4;
    f32x4 csv[3];
#pragma unroll
    for (int j = 0; j < 3; ++j) csv[j] = *(LDS_AS f32x4*)((LDS_AS char*)smem + K3_MISC + 1024 + ((w * 3 + j) * 16 + 4 * g) * 4);
    LDS_AS char* xp = (LDS_AS char*)smem + lane * 16;
#pragma unroll
    for (int tt = 0; tt < 8; ++tt) {
        f32x2 st = stl[tt * 16 + (lane & 15)];
#pragma unroll
        for (int j = 0; j < 3; ++j) {
            float g0 = fmaf(-st[0], csv[j][0], acc[tt][j][0]);
            float g1 = fmaf(-st[0], csv[j][1], acc[tt][j][1]);
            float u0 = fmaf(-st[0], csv[j][2], acc[tt][j][2]);
            float u1 = fmaf(-st[0], csv[j][3], acc[tt][j][3]);
            float o0 = fmaxf(g0, 0.f) * u0 * st[1], o1 = fmaxf(g1, 0.f) * u1 * st[1];
            const int T = 3 * w + j;
            *(LDS_AS u32*)(xp + (tt * 3 + (T >> 2)) * 1024 + (T & 3) * 4) = pk2(o0, o1);
        }
    }
    __syncthreads();
#pragma unroll
    for (int i = 0; i < 6; ++i) {
        const int fr = w * 6 + i, tt = fr / 3, sp = fr % 3;
        u32x4 v = *(LDS_AS u32x4*)(xp + fr * 1024);
        *(u32x4*)(Hf + ((size_t)((tb * 8 + tt) * 48 + nb * 3 + sp) * 64 + lane) * 8) = v;
    }
}

#define K4_RES 61440
#define K4_LDS (K4_RES + 12288)
__global__ __launch_bounds__(256, 1) void k4_gemm2(const u16* __restrict__ Hf, const u16* __restrict__ wd_t, const u16* __restrict__ hT,
                                                   u16* __restrict__ out) {
    extern __shared__ __attribute__((aligned(16))) char smem[];
    const int tid = threadIdx.x, lane = tid & 63;
    const int w = __builtin_amdgcn_readfirstlane(tid >> 6);
    const int xcd = blockIdx.x & 7, idx = blockIdx.x >> 3;
    const int tb = (xcd & 3) * 8 + (idx & 7), db = (xcd >> 2) * 4 + (idx >> 3);
    const u16* hsrc = Hf + ((size_t)(tb * 4) * 48 + w * 12) * 512 + lane * 8;
    const u16* wsrc = wd_t + ((size_t)(db * 48 + w * 12) * 5) * 512 + lane * 8;
    const int ks0 = (db * 80) >> 5;
    u32x4 rs[3];
    {
        const int tti = tb * 4 + w;
#pragma unroll
        for (int i = 0; i < 3; ++i) {
            int ks = ks0 + i; ks = ks > 19 ? 19 : ks;
            rs[i] = *(const u32x4*)(hT + ((size_t)(((tti >> 3) * 20 + ks) * 8 + (tti & 7)) * 64 + lane) * 8);
        }
    }
    u32x4 Hh[12][4], Ww[12][5];
#define K4_LOAD(s) do { \
        _Pragma("unroll") for (int i = 0; i < 4; ++i) Hh[s][i] = *(const u32x4*)(hsrc + (size_t)i * (48 * 512) + (s) * 512); \
        _Pragma("unroll") for (int i = 0; i < 5; ++i) Ww[s][i] = *(const u32x4*)(wsrc + ((s) * 5 + i) * 512); \
    } while (0)
#pragma unroll
    for (int s = 0; s < 6; ++s) K4_LOAD(s);
    __builtin_amdgcn_sched_barrier(0);
#pragma unroll
    for (int i = 0; i < 3; ++i) *(LDS_AS u32x4*)((LDS_AS char*)smem + K4_RES + (w * 3 + i) * 1024 + lane * 16) = rs[i];
    f32x4 acc[4][5];
#pragma unroll
    for (int i = 0; i < 4; ++i)
#pragma unroll
        for (int j = 0; j < 5; ++j) acc[i][j] = (f32x4){0.f, 0.f, 0.f, 0.f};
#pragma unroll
    for (int s = 0; s < 12; ++s) {
        __builtin_amdgcn_sched_barrier(0);
        if (s + 6 < 12) K4_LOAD(s + 6);
        __builtin_amdgcn_sched_barrier(0);
#pragma unroll
        for (int i = 0; i < 4; ++i)
#pragma unroll
            for (int j = 0; j < 5; ++j) acc[i][j] = mfma16(Ww[s][j], Hh[s][i], acc[i][j]);
    }
    LDS_AS char* rp = (LDS_AS char*)smem + lane * 16;
#pragma unroll
    for (int i = 0; i < 4; ++i) {
        if (i != w) {
            const int sl = i * 3 + (w > i ? w - 1 : w);
#pragma unroll
            for (int j = 0; j < 5; ++j) *(LDS_AS f32x4*)(rp + (sl * 5 + j) * 1024) = acc[i][j];
        }
    }
    __syncthreads();
    f32x4 fin[5];
#pragma unroll
    for (int j = 0; j < 5; ++j) fin[j] = (w == 0) ? acc[0][j] : (w == 1) ? acc[1][j] : (w == 2) ? acc[2][j] : acc[3][j];
#pragma unroll
    for (int sI = 0; sI < 3; ++sI)
#pragma unroll
        for (int j = 0; j < 5; ++j) fin[j] += *(LDS_AS f32x4*)(rp + ((w * 3 + sI) * 5 + j) * 1024);
    const int g = lane >> 4, t = tb * 64 + w * 16 + (lane & 15);
    const int half0 = (db * 5) & 1;
    LDS_AS char* resp = (LDS_AS char*)smem + K4_RES + w * 3072 + half0 * 512 + lane * 8;
#pragma unroll
    for (int j = 0; j < 5; ++j) {
        u32x2 r = trread(resp + j * 512);
        u32x2 o = {pk2(fin[j][0] + bflo(r[0]), fin[j][1] + bfhi(r[0])), pk2(fin[j][2] + bflo(r[1]), fin[j][3] + bfhi(r[1]))};
        *(u32x2*)(out + (size_t)t * D_ + db * 80 + j * 16 + 4 * g) = o;
    }
}

extern "C" void kernel_launch(void* const* d_in, const int* in_sizes, int n_in,
                              void* d_out, int out_size, void* d_ws, size_t ws_size,
                              hipStream_t stream) {
    const u16* q = (const u16*)d_in[0];
    const u16* k = (const u16*)d_in[1];
    const u16* v = (const u16*)d_in[2];
    const u16* sk = (const u16*)d_in[3];
    const u16* hk = (const u16*)d_in[4];
    const u16* Dv = (const u16*)d_in[5];
    const u16* wg = (const u16*)d_in[7];
    const u16* wu = (const u16*)d_in[8];
    const u16* wd = (const u16*)d_in[9];
    char* ws = (char*)d_ws;
    (void)hipFuncSetAttribute((const void*)k2_conv, hipFuncAttributeMaxDynamicSharedMemorySize, K2_LDS);
    (void)hipFuncSetAttribute((const void*)k3_gemm1, hipFuncAttributeMaxDynamicSharedMemorySize, K3_LDS);
    (void)hipFuncSetAttribute((const void*)k4_gemm2, hipFuncAttributeMaxDynamicSharedMemorySize, K4_LDS);
    k1_prep<<<576, 256, 0, stream>>>(q, k, v, sk, (u16*)(ws + WS_UR), (u16*)(ws + WS_XT), (f32x2*)(ws + WS_ST1));
    k2_conv<<<250, 256, K2_LDS, stream>>>(hk, Dv, (const u16*)(ws + WS_UR), (const u16*)(ws + WS_XT), (const f32x2*)(ws + WS_ST1),
                                          (u16*)(ws + WS_HT), wg, wu, wd, (u16*)(ws + WS_WGU), (float*)(ws + WS_CS), (u16*)(ws + WS_WD));
    k3_gemm1<<<256, 256, K3_LDS, stream>>>((const u16*)(ws + WS_HT), (const u16*)(ws + WS_WGU), (const float*)(ws + WS_CS), (u16*)(ws + WS_HF));
    k4_gemm2<<<256, 256, K4_LDS, stream>>>((const u16*)(ws + WS_HF), (const u16*)(ws + WS_WD), (const u16*)(ws + WS_HT), (u16*)d_out);
}
```

```cpp
#include <hip/hip_runtime.h>
#include <hip/hip_bf16.h>
#include <stdint.h>

typedef unsigned short u16;
typedef unsigned int u32;
typedef __attribute__((ext_vector_type(2))) unsigned int u32x2;
typedef __attribute__((ext_vector_type(4))) unsigned int u32x4;
typedef __attribute__((ext_vector_type(4))) float f32x4;
typedef __attribute__((ext_vector_type(2))) float f32x2;
typedef __attribute__((ext_vector_type(8))) __bf16 bf16x8;
typedef __attribute__((ext_vector_type(2))) __bf16 bf16x2;
typedef __attribute__((ext_vector_type(4))) short s16x4;
#define LDS_AS __attribute__((address_space(3)))
#define GLB_AS __attribute__((address_space(1)))

#define S_ 2048
#define D_ 640
#define F_ 1536
#define UR0K1 0
#define URS 2112
#define UR0_OFF 2

#define WS_FLG  0
#define FL_A    0
#define FL_B    (224 * 128)
#define FL_H    (448 * 128)
#define FLG_BYTES (704 * 128)
#define WS_UR   FLG_BYTES
#define WS_XT   (WS_UR + D_ * URS * 2)
#define WS_ST1  (WS_XT + D_ * S_ * 2)
#define WS_HT   (WS_ST1 + S_ * 8)
#define WS_WGU  (WS_HT + D_ * S_ * 2)
#define WS_CS   (WS_WGU + D_ * F_ * 2 * 2)
#define WS_WD   (WS_CS + 10 * 3072 * 4)
#define WS_HF   (WS_WD + D_ * F_ * 2)
#define WS_END  (WS_HF + S_ * F_ * 2)

__device__ __forceinline__ float bflo(u32 x) { return __uint_as_float(x << 16); }
__device__ __forceinline__ float bfhi(u32 x) { return __uint_as_float(x & 0xffff0000u); }
__device__ __forceinline__ u32 pk2(float lo, float hi) {
    bf16x2 r; r[0] = (__bf16)lo; r[1] = (__bf16)hi;
    return __builtin_bit_cast(u32, r);
}
__device__ __forceinline__ f32x4 mfma16(u32x4 a, u32x4 b, f32x4 c) {
    return __builtin_amdgcn_mfma_f32_16x16x32_bf16(__builtin_bit_cast(bf16x8, a), __builtin_bit_cast(bf16x8, b), c, 0, 0, 0);
}
__device__ __forceinline__ float dot2(u32 a, u32 b, float c) {
    return __builtin_amdgcn_fdot2_f32_bf16(__builtin_bit_cast(bf16x2, a), __builtin_bit_cast(bf16x2, b), c, false);
}
__device__ __forceinline__ u32x4 cat2(u32x2 a, u32x2 b) { u32x4 r = {a[0], a[1], b[0], b[1]}; return r; }
#define SB __builtin_amdgcn_sched_barrier(0)
#define BAR() do { asm volatile("" ::: "memory"); __builtin_amdgcn_s_barrier(); asm volatile("" ::: "memory"); } while (0)
#define DMA(voff, sbase, ldsS, ldsI, FL) asm volatile("s_add_u32 m0, %2, %3\n\ts_nop 0\n\tglobal_load_lds_dwordx4 %0, %1" FL :: "v"(voff), "s"(sbase), "s"(ldsS), "i"(ldsI) : "memory", "scc")
#define DMA_M0(ldsS, ldsI) asm volatile("s_add_u32 m0, %0, %1" :: "s"(ldsS), "i"(ldsI) : "memory", "scc")
#define DMA_GO(voff, sbase, imm, FL) asm volatile("global_load_lds_dwordx4 %0, %1 offset:%2" FL :: "v"(voff), "s"(sbase), "i"(imm) : "memory")
#define DMA_GO1(voff, sbase, imm, FL) asm volatile("s_nop 0\n\tglobal_load_lds_dwordx4 %0, %1 offset:%2" FL :: "v"(voff), "s"(sbase), "i"(imm) : "memory")
#define EPI_P(a, b, c, d, p0, p1, p2, p3) asm volatile("v_add_f32 %0, %0, %4\n\tv_add_f32 %1, %1, %5\n\tv_add_f32 %2, %2, %6\n\tv_add_f32 %3, %3, %7" : "+v"(a), "+v"(b), "+v"(c), "+v"(d) : "v"(p0), "v"(p1), "v"(p2), "v"(p3))
#define EPI_A(a, c, nm, c0, c2) asm volatile("v_fmac_f32 %0, %2, %3\n\tv_fmac_f32 %1, %2, %4" : "+v"(a), "+v"(c) : "v"(nm), "v"(c0), "v"(c2))
#define EPI_B(a, c, r) asm volatile("v_max_f32 %0, 0, %0\n\tv_mul_f32 %1, %2, %1" : "+v"(a), "+v"(c) : "v"(r))
#define EPI_C(a, b, c, d) asm volatile("v_mul_f32 %0, %0, %2\n\tv_mul_f32 %1, %1, %3" : "+v"(a), "+v"(b) : "v"(c), "v"(d))
#define EPI_D(a, b, addr, imm) asm volatile("v_cvt_pk_bf16_f32 %0, %0, %1\n\tds_write_b32 %2, %0 offset:%3" : "+v"(a) : "v"(b), "v"(addr), "i"(imm) : "memory")
#define LRD2S(dst, addr, o0, o1) asm volatile("ds_read2st64_b32 %0, %1 offset0:%2 offset1:%3" : "=v"(dst) : "v"(addr), "i"(o0), "i"(o1) : "memory")
#define GLD4(dst, C, voff, sbase, imm, FL) asm volatile("global_load_dwordx4 %0, %1, %2 offset:%3" FL : "=" C(dst) : "v"(voff), "s"(sbase), "i"(imm) : "memory")
#define GLD1(dst, voff, sbase, imm, FL) asm volatile("global_load_dword %0, %1, %2 offset:%3" FL : "=v"(dst) : "v"(voff), "s"(sbase), "i"(imm) : "memory")
#define GST4(voff, src, sbase, imm) asm volatile("global_store_dwordx4 %0, %1, %2 offset:%3 sc1\n\ts_nop 1" :: "v"(voff), "v"(src), "s"(sbase), "i"(imm) : "memory")
#define GST2(voff, src, sbase) asm volatile("global_store_dwordx2 %0, %1, %2 sc1" :: "v"(voff), "v"(src), "s"(sbase) : "memory")
#define LRD4(dst, C, addr, imm) asm volatile("ds_read_b128 %0, %1 offset:%2" : "=" C(dst) : "v"(addr), "i"(imm) : "memory")
#define LRD1(dst, addr, imm) asm volatile("ds_read_b32 %0, %1 offset:%2" : "=v"(dst) : "v"(addr), "i"(imm) : "memory")
#define LRD2(dst, addr, imm) asm volatile("ds_read_b64 %0, %1 offset:%2" : "=v"(dst) : "v"(addr), "i"(imm) : "memory")
#define LRT2(dst, addr, imm) asm volatile("ds_read_b64_tr_b16 %0, %1 offset:%2" : "=v"(dst) : "v"(addr), "i"(imm) : "memory")
#define LWR1(addr, src, imm) asm volatile("ds_write_b32 %0, %1 offset:%2" :: "v"(addr), "v"(src), "i"(imm) : "memory")
#define LWR2(addr, src, imm) asm volatile("ds_write_b64 %0, %1 offset:%2" :: "v"(addr), "v"(src), "i"(imm) : "memory")
#define LWR4(addr, src, imm) asm volatile("ds_write_b128 %0, %1 offset:%2" :: "v"(addr), "v"(src), "i"(imm) : "memory")
#define MF(acc, A, CA, B, CB) asm volatile("v_mfma_f32_16x16x32_bf16 %0, %1, %2, %0" : "+v"(acc) : CA(A), CB(B))
#define MF0(acc, A, CA, B, CB) asm volatile("v_mfma_f32_16x16x32_bf16 %0, %1, %2, 0" : "=v"(acc) : CA(A), CB(B))
#define DOT(acc, A, B) asm volatile("v_dot2c_f32_bf16 %0, %1, %2" : "+v"(acc) : "v"(A), "v"(B))
#define PIN(x) asm volatile("" : "+v"(x))
#define MFNOP(x) asm volatile("s_nop 7" : "+v"(x))
#define FLAG_SET(p) __hip_atomic_store((GLB_AS u32*)(p), 1u, __ATOMIC_RELAXED, __HIP_MEMORY_SCOPE_AGENT)

#define STAMP_DECL
#define STAMP(i)
#define STAMP_END(kid)
#define FINE_DECL
#define FINE(i)
#define FINE_END()

__device__ __forceinline__ float dpp_add(float v, int dummy) { return v; }
template <int CTRL> __device__ __forceinline__ float dpp_addc(float v) {
    return v + __builtin_bit_cast(float, __builtin_amdgcn_update_dpp(0, __builtin_bit_cast(int, v), CTRL, 0xf, 0xf, true));
}
__device__ __forceinline__ float rows_sum(float v) {
    u32 x = __float_as_uint(v);
    auto a = __builtin_amdgcn_permlane16_swap(x, x, false, false);
    v = __uint_as_float(a[0]) + __uint_as_float(a[1]);
    x = __float_as_uint(v);
    auto b = __builtin_amdgcn_permlane32_swap(x, x, false, false);
    return __uint_as_float(b[0]) + __uint_as_float(b[1]);
}
__device__ __forceinline__ float wave_sum(float v) {
    v = dpp_addc<0xB1>(v); v = dpp_addc<0x4E>(v); v = dpp_addc<0x141>(v); v = dpp_addc<0x140>(v);
    return rows_sum(v);
}
#define K1_NT 1
template <typename T> __device__ __forceinline__ T ldin(const T* p) {
    return __builtin_nontemporal_load(p);
}

#define K1_SYM "k1_prep"
#define KM_SYM "k_main"
#define K1_TR 320
#define K1_LN 256
#define K1_GU 120
extern "C" __global__ __launch_bounds__(256) void k1_prep(const u16* __restrict__ x, const u16* __restrict__ k, const u16* __restrict__ v,
                                                          const u16* __restrict__ sk, const u16* __restrict__ wg, const u16* __restrict__ wu, char* __restrict__ ws) {
    __shared__ __attribute__((aligned(16))) char smem_[32768 + 1024];
    LDS_AS char* sm = (LDS_AS char*)smem_;
    const int tid = threadIdx.x, lane = tid & 63, w = __builtin_amdgcn_readfirstlane(tid >> 6);
    int b = blockIdx.x;
    STAMP_DECL; STAMP(0);
    u32 pf = 0;
    {
        const char* pc; int o1, o2, o3;
        asm volatile("s_getpc_b64 %0\n.Lpc%=:\n\ts_mov_b32 %1, " K1_SYM "-.Lpc%=\n\ts_mov_b32 %2, " KM_SYM "-.Lpc%=\n\ts_mov_b32 %3, .Lkm_end-" KM_SYM : "=s"(pc), "=s"(o1), "=s"(o2), "=s"(o3));
        const int q = (b >> 3) * 256 + tid;
        if (b < 8) { if (tid < 96) pf = *(const GLB_AS u32*)(pc + o1 + tid * 64); }
        else if ((q - 256) * 128 < o3) pf = *(const GLB_AS u32*)(pc + o2 + (q - 256) * 128);
    }
    u16* uR = (u16*)(ws + WS_UR); u16* xT = (u16*)(ws + WS_XT); f32x2* st1 = (f32x2*)(ws + WS_ST1);
    if (b < K1_TR) {
        const int tb = b / 10, cb = b % 10;
        const int cp = tid & 31, tg = tid >> 5;
        const int c0 = cb * 64 + cp * 2, t0 = tb * 64 + tg * 8;
        u32 kk[12], vv[9], xx[8];
#pragma unroll
        for (int j = 1 - UR0K1; j < 12; ++j) {
            int t = t0 - 4 + j;
            kk[j] = ldin((const u32*)(k + (size_t)(t < 0 ? 0 : t) * D_ + c0));
        }
#pragma unroll
        for (int j = 1 - UR0K1; j < 9; ++j) { int t = t0 - 1 + j; vv[j] = ldin((const u32*)(v + (size_t)(t < 0 ? 0 : t) * D_ + c0)); }
        const int xb = t0 >> 5, par = tg & 1, half = (tg >> 1) & 1;
#pragma unroll
        for (int j = 0; j < 8; ++j) xx[j] = ldin((const u32*)(x + (size_t)(xb * 32 + 16 * half + 2 * j + par) * D_ + c0));
        u32x4 wv = *(const u32x4*)(sk + c0 * 4);
        if (t0 == 0) { kk[0] = 0; kk[1] = 0; kk[2] = 0; kk[3] = 0; vv[0] = 0; }
        float wa[4] = {bflo(wv[0]), bfhi(wv[0]), bflo(wv[1]), bfhi(wv[1])};
        float wb[4] = {bflo(wv[2]), bfhi(wv[2]), bflo(wv[3]), bfhi(wv[3])};
        float ua[9], ub[9];
#pragma unroll
        for (int j = 1 - UR0K1; j < 9; ++j) {
            float a = bflo(kk[j]) * wa[3], c = bfhi(kk[j]) * wb[3];
            a = fmaf(bflo(kk[j + 1]), wa[2], a); c = fmaf(bfhi(kk[j + 1]), wb[2], c);
            a = fmaf(bflo(kk[j + 2]), wa[1], a); c = fmaf(bfhi(kk[j + 2]), wb[1], c);
            a = fmaf(bflo(kk[j + 3]), wa[0], a); c = fmaf(bfhi(kk[j + 3]), wb[0], c);
            ua[j] = a * bflo(vv[j]); ub[j] = c * bfhi(vv[j]);
        }
        u32x4 oa, ob, xa, xc, pa, pb;
#pragma unroll
        for (int j = 0; j < 4; ++j) {
            oa[j] = pk2(ua[8 - 2 * j], ua[7 - 2 * j]);
            ob[j] = pk2(ub[8 - 2 * j], ub[7 - 2 * j]);
            xa[j] = __builtin_amdgcn_perm(xx[2 * j + 1], xx[2 * j], 0x05040100u);
            xc[j] = __builtin_amdgcn_perm(xx[2 * j + 1], xx[2 * j], 0x07060302u);
        }
        const int cu = 7 - tg, cx = (tg & 4) + par * 2 + half;
        *(LDS_AS u32x4*)(sm + (2 * cp) * 128 + ((cu ^ ((2 * cp) & 7)) * 16)) = oa;
        *(LDS_AS u32x4*)(sm + (2 * cp + 1) * 128 + ((cu ^ ((2 * cp + 1) & 7)) * 16)) = ob;
        *(LDS_AS u32x4*)(sm + 8192 + (2 * cp) * 128 + ((cx ^ ((2 * cp) & 7)) * 16)) = xa;
        *(LDS_AS u32x4*)(sm + 8192 + (2 * cp + 1) * 128 + ((cx ^ ((2 * cp + 1) & 7)) * 16)) = xc;
        __syncthreads();
#pragma unroll
        for (int i = 0; i < 2; ++i) {
            const int ch = i * 32 + (tid >> 3), ck = tid & 7;
            const u32x4 pu = *(LDS_AS u32x4*)(sm + ch * 128 + ((ck ^ (ch & 7)) * 16));
            const u32x4 px = *(LDS_AS u32x4*)(sm + 8192 + ch * 128 + ((ck ^ (ch & 7)) * 16));
            *(u32x4*)(uR + (size_t)(cb * 64 + ch) * URS + (S_ - 64 - tb * 64) + ck * 8) = pu;
            *(u32x4*)(xT + (size_t)(cb * 64 + ch) * S_ + tb * 64 + ck * 8) = px;
        }
        if (!UR0K1 && tb == 0 && tid < 64) { u32x4 z = {0u, 0u, 0u, 0u}; *(u32x4*)(uR + (size_t)(cb * 64 + tid) * URS + S_) = z; }
    } else if (b < K1_TR + K1_GU) {
        b -= K1_TR;
        const int cpb = b % 12, ksg = b / 12;
        const int col = (cpb * 64 + lane) * 2;
        u32 G[2][8], U[2][8];
#pragma unroll
        for (int s = 0; s < 2; ++s)
#pragma unroll
            for (int j = 0; j < 8; ++j) {
                const int kr = (ksg * 2 + s) * 32 + 16 * (j >> 2) + 4 * w + (j & 3);
                G[s][j] = ldin((const u32*)(wg + (size_t)kr * F_ + col));
                U[s][j] = ldin((const u32*)(wu + (size_t)kr * F_ + col));
            }
        const int sw = ((lane >> 1) & 1) | (((lane >> 2) & 1) << 1);
        LDS_AS char* ip = sm + (lane >> 2) * 1024 + (16 * w + 4 * (lane & 3)) * 16;
#pragma unroll
        for (int s = 0; s < 2; ++s) {
            u32x4 r0, r1, r2, r3;
#pragma unroll
            for (int j = 0; j < 4; ++j) {
                r0[j] = __builtin_amdgcn_perm(G[s][2 * j + 1], G[s][2 * j], 0x05040100u);
                r1[j] = __builtin_amdgcn_perm(G[s][2 * j + 1], G[s][2 * j], 0x07060302u);
                r2[j] = __builtin_amdgcn_perm(U[s][2 * j + 1], U[s][2 * j], 0x05040100u);
                r3[j] = __builtin_amdgcn_perm(U[s][2 * j + 1], U[s][2 * j], 0x07060302u);
            }
            *(LDS_AS u32x4*)(ip + s * 16384 + (0 ^ sw) * 16) = r0; *(LDS_AS u32x4*)(ip + s * 16384 + (1 ^ sw) * 16) = r1;
            *(LDS_AS u32x4*)(ip + s * 16384 + (2 ^ sw) * 16) = r2; *(LDS_AS u32x4*)(ip + s * 16384 + (3 ^ sw) * 16) = r3;
        }
        __syncthreads();
        const u32x4 ones4 = {0x3f803f80u, 0x3f803f80u, 0x3f803f80u, 0x3f803f80u};
#pragma unroll
        for (int i = 0; i < 4; ++i) {
            const int T = cpb * 16 + w * 4 + i;
            f32x4 cs = {0.f, 0.f, 0.f, 0.f};
#pragma unroll
            for (int s = 0; s < 2; ++s) {
                const u32x4 o = *(LDS_AS u32x4*)(sm + s * 16384 + (w * 4 + i) * 1024 + (lane ^ (((lane >> 3) & 1) | ((i & 1) << 1))) * 16);
                *(u32x4*)(ws + WS_WGU + (size_t)(((T / 3) * 20 + ksg * 2 + s) * 3 + T % 3) * 1024 + lane * 16) = o;
                cs = mfma16(o, ones4, cs);
            }
            if ((lane & 15) == 0) *(LDS_AS f32x4*)(sm + 32768 + (w * 4 + i) * 64 + (lane >> 4) * 16) = cs;
        }
        if (lane < 16) {
            const f32x4 t = *(LDS_AS f32x4*)(sm + 32768 + w * 256 + lane * 16);
            *(f32x4*)(ws + WS_CS + (ksg * 3072 + cpb * 256 + w * 64) * 4 + lane * 16) = t;
        }
    } else {
        b -= K1_TR + K1_GU;
        const int t0 = b * 8 + w * 2;
        u32 r[2][5];
#pragma unroll
        for (int i = 0; i < 2; ++i)
#pragma unroll
            for (int j = 0; j < 5; ++j) r[i][j] = ldin((const u32*)(x + (size_t)(t0 + i) * D_ + (j * 64 + lane) * 2));
        if (b < FLG_BYTES / 4096) { u32x4 z = {0u, 0u, 0u, 0u}; *(u32x4*)(ws + WS_FLG + (b * 256 + tid) * 16) = z; }
#pragma unroll
        for (int i = 0; i < 2; ++i) {
            float p = bflo(__builtin_amdgcn_readfirstlane(r[i][0]));
            float s = 0.f, q = 0.f;
#pragma unroll
            for (int j = 0; j < 5; ++j) {
                float a = bflo(r[i][j]) - p, c = bfhi(r[i][j]) - p;
                s += a + c; q = fmaf(a, a, q); q = fmaf(c, c, q);
            }
            s = wave_sum(s); q = wave_sum(q);
            float dm = s * (1.f / D_);
            float var = fmaxf(q * (1.f / D_) - dm * dm, 0.f);
            float rs = rsqrtf(var + 1e-5f);
            const int t = t0 + i, slot = (t & ~31) + (t & 1) * 16 + ((t & 31) >> 1);
            if (lane == 0) { f32x2 o; o[0] = rs; o[1] = -(p + dm) * rs; st1[slot] = o; }
        }
    }
    asm volatile("" :: "v"(pf));
    STAMP_END(0);
}

#define CV_US  4640
#define CV_X   9280
#define CV_ST  13376
#define CV_H   81920
#define CV_CPY 4160
#define CV_HS  16640
#define CV_HO  (CV_H + 4 * CV_HS)
#define L_CNT  (CV_HO + 8192)
#define G_HX   81920
#define G_RES  (G_HX + 12288)
#define G_ST   (G_RES + 12288)
#define G_CS   (G_ST + 1024)
#define G_CF   (G_CS + 3072)
#define G_VL   (G_CF + 768)
#define MAIN_LDS (G_VL + 49152 + 16)
#undef L_CNT
#define L_CNT  (G_VL + 49152)
#define ARRIVE(i, flag) do { if (lane == 0) { if (__hip_atomic_fetch_add((LDS_AS u32*)(sm + L_CNT) + (i), 1u, __ATOMIC_RELAXED, __HIP_MEMORY_SCOPE_WORKGROUP) == 3u) FLAG_SET(flag); } \
        asm volatile("s_waitcnt lgkmcnt(0)" ::: "memory"); } while (0)
#define HADD(i) do { if (lane == 0) (void)__hip_atomic_fetch_add((LDS_AS u32*)(sm + L_CNT) + (i), 1u, __ATOMIC_RELAXED, __HIP_MEMORY_SCOPE_WORKGROUP); } while (0)
__device__ __forceinline__ bool hwait(LDS_AS u32* p, int n) {
#pragma unroll 1
    for (int it = 0; it < n; ++it) {
        if (__builtin_amdgcn_readfirstlane(__hip_atomic_load(p, __ATOMIC_RELAXED, __HIP_MEMORY_SCOPE_WORKGROUP)) == 4u) return true;
        __builtin_amdgcn_s_sleep(1);
    }
    return false;
}
#define HWAIT(i, n) hwait((LDS_AS u32*)(sm + L_CNT) + (i), n)
__device__ __forceinline__ bool try1(const char* base, u32 voff) {
    u32 f;
    asm volatile("global_load_dword %0, %1, %2 sc1\n\ts_waitcnt vmcnt(0)" : "=&v"(f) : "v"(voff), "s"(base) : "memory");
    return __all(f == 1u);
}
#define TRY1(base, voff) try1(base, voff)
#define HDMA9() do { DMA_M0(gw, 0); DMA_GO1(vo16, hsA, 0, " sc1"); DMA_GO(vo16, hsA, 1024, " sc1"); DMA_GO(vo16, hsA, 2048, " sc1"); DMA_GO(vo16, hsA, 3072, " sc1"); \
        DMA_M0(gw, 4096); DMA_GO1(vo16, hsA + 4096, 0, " sc1"); DMA_GO(vo16, hsA + 4096, 1024, " sc1"); DMA_GO(vo16, hsA + 4096, 2048, " sc1"); DMA_GO(vo16, hsA + 4096, 3072, " sc1"); \
        DMA_M0(gw, 8192); DMA_GO1(vo16, hsA + 8192, 0, " sc1"); } while (0)
#define STAT(s, x) do { float lo_, hi_; asm volatile("v_lshlrev_b32 %4, 16, %6\n\tv_and_b32 %5, 0xffff0000, %6\n\tv_add_f32 %0, %0, %4\n\tv_add_f32 %1, %1, %5\n\tv_fmac_f32 %2, %4, %4\n\tv_fmac_f32 %3, %5, %5" \
        : "+v"(s[0]), "+v"(s[1]), "+v"(s[2]), "+v"(s[3]), "=&v"(lo_), "=&v"(hi_) : "v"(x)); } while (0)

__device__ __forceinline__ void spin1(const char* base, u32 voff, int slow) {
#pragma unroll 1
    for (int it = 0; it < (1 << 16); ++it) {
        u32 f;
        asm volatile("global_load_dword %0, %1, %2 sc1\n\ts_waitcnt vmcnt(0)" : "=&v"(f) : "v"(voff), "s"(base) : "memory");
        if (__all(f == 1u)) return;
        if (slow) __builtin_amdgcn_s_sleep(12); else __builtin_amdgcn_s_sleep(2);
    }
}
#define CHECK1(f, base, voff) do { if (!__all((f) == 1u)) spin1(base, voff, 0); } while (0)

extern "C" __global__ __launch_bounds__(512, 2) void k_main(const u16* __restrict__ hk, const u16* __restrict__ Dv,
                                                            const u16* __restrict__ wd,
                                                            char* __restrict__ ws, u16* __restrict__ out) {
    extern __shared__ __attribute__((aligned(16))) char smem[];
    LDS_AS char* sm = (LDS_AS char*)smem;
    STAMP_DECL; FINE_DECL; STAMP(14);
    asm volatile("" :: "s"(hk), "s"(Dv), "s"(wd), "s"(ws), "s"(out));
    const int tid = threadIdx.x, lane = tid & 63;
    const int w8 = __builtin_amdgcn_readfirstlane(tid >> 6), w = w8 & 3, kh = w8 >> 2;
    const int xcd = blockIdx.x & 7, idx = blockIdx.x >> 3;
    const int cv = idx < 20 ? xcd * 20 + idx : -1;
    const int a = lane & 15, g = lane >> 4;
    const u32 lb = (u32)(size_t)sm;
    const u32 vo16 = lane * 16;
    const int tbp = (xcd & 1) * 8 + (idx & 7), nb = (xcd >> 1) * 4 + (idx >> 3);
    const int tq = 2 * tbp + (nb >> 3), db = nb & 7;
    const u32 voGA = (w * 40 + (lane < 40 ? lane : 39)) * 128, voGB = voGA;
    const u32 voGD = (160 + ((lane & 1) ? (db * 80 + 79) >> 7 : (db * 80) >> 7) * 12 + ((lane >> 1) % 12)) * 128;
    u32 fa_;
    const char* wsrc = ws + WS_WGU + (size_t)(nb * 4 + w) * (20 * 3072);
    const int d = cv * 4 + w, pi = xcd * 12 + idx - 20;
    const int rot = w;
    const u32 gw = lb + w * 20480, gw3 = lb + G_RES + w * 3072;
    const char* hsA = ws + WS_HT + (size_t)(tbp * 4 + w) * 20480;
    const char* hsB = hsA + 64 * 20480;
    const char* vsrc = ws + WS_WD + (size_t)(db * 48 + w8 * 6) * 5120;
    const char* vsrc0 = ws + WS_WD + (size_t)(db * 48 + w * 6) * 5120;
    const u32 vlw = lb + G_VL + w * 12288, vlr = vlw + lane * 16;
    const char* csrc = ws + WS_CS + nb * 768 + w * 12288;
    const u32 voC = (lane < 48 ? lane : 47) * 16, voF1 = (lane & 15) * 128;
    const char* hoA = ws + WS_HF + ((size_t)(tbp * 4 + w) * 48 + nb * 3) * 1024;
    const char* fsA = ws + WS_HF + ((size_t)(tq * 2) * 48 + w8 * 6) * 1024;
    const char* fsB = fsA + 64 * 49152;
    char* flH = ws + FL_H + tbp * 2048;
    const int ttr = 2 * tq + (w & 1) + (w >> 1) * 64;
    const int ks0 = (db * 80) >> 5;
    const char* rsrc0 = ws + (u32)__builtin_amdgcn_readfirstlane(WS_HT + (ttr * 20 + ks0) * 1024);
    const char* rsrc1 = rsrc0 + 1024;
    const char* rsrc2 = rsrc0 + (u32)__builtin_amdgcn_readfirstlane(ks0 + 2 < 20 ? 2048 : 1024);
    const u32 hp0 = lb + lane * 8 + ((0 + rot) & 3) * 20480, hp1 = lb + lane * 8 + ((1 + rot) & 3) * 20480;
    const u32 hp2 = lb + lane * 8 + ((2 + rot) & 3) * 20480, hp3 = lb + lane * 8 + ((3 + rot) & 3) * 20480;
    const u32 sp0 = lb + G_ST + (((0 + rot) & 3) * 16 + a) * 8, sp1 = lb + G_ST + (((1 + rot) & 3) * 16 + a) * 8;
    const u32 sp2 = lb + G_ST + (((2 + rot) & 3) * 16 + a) * 8, sp3 = lb + G_ST + (((3 + rot) & 3) * 16 + a) * 8;
    const u32 cp = lb + G_CF + (w * 48 + 4 * g) * 4;
    const u32 csw = lb + G_CS + w * 768 + lane * 16, csr = lb + G_CS + (w * 48 + (lane < 48 ? lane : 47)) * 4, csf = lb + G_CF + (w * 48 + lane) * 4;
    const u32 xp0 = lb + G_HX + ((0 + rot) & 3) * 3072 + w * 768 + lane * 4, xp1 = lb + G_HX + ((1 + rot) & 3) * 3072 + w * 768 + lane * 4;
    const u32 xp2 = lb + G_HX + ((2 + rot) & 3) * 3072 + w * 768 + lane * 4, xp3 = lb + G_HX + ((3 + rot) & 3) * 3072 + w * 768 + lane * 4;
    const u32 xr = lb + G_HX + w * 3072 + lane * 4;
    const u32 rpw = lb + w8 * 10240 + lane * 16;
#define FINISH1(part, u) do { \
        const int ti = (u) >= 5 ? 1 : 0, dj = (u) - 5 * ti; \
        LDS_AS char* rp = sm + (u) * 1024 + lane * 16; \
        f32x4 f = ((*(LDS_AS f32x4*)(rp) + *(LDS_AS f32x4*)(rp + 10240)) + (*(LDS_AS f32x4*)(rp + 20480) + *(LDS_AS f32x4*)(rp + 30720))) \
                + ((*(LDS_AS f32x4*)(rp + 40960) + *(LDS_AS f32x4*)(rp + 51200)) + (*(LDS_AS f32x4*)(rp + 61440) + *(LDS_AS f32x4*)(rp + 71680))); \
        s16x4 rr_ = __builtin_amdgcn_ds_read_tr16_b64_v4i16((LDS_AS s16x4*)(sm + G_RES + ((part) * 2 + ti) * 3072 + (((db * 5) & 1) + dj) * 512 + lane * 8)); \
        u32x2 rr = __builtin_bit_cast(u32x2, rr_); \
        u32x2 o = {pk2(f[0] + bflo(rr[0]), f[1] + bfhi(rr[0])), pk2(f[2] + bflo(rr[1]), f[3] + bfhi(rr[1]))}; \
        const int t = (part) * 1024 + 32 * tq + 2 * a + ti; \
        *(u32x2*)(out + (size_t)t * D_ + db * 80 + dj * 16 + 4 * g) = o; \
    } while (0)
#define HB_CHECK() do { \
        _Pragma("unroll 1") for (int it = 0; it < (1 << 14); ++it) { \
            u32 mx = 0; \
            _Pragma("unroll") for (int i = 0; i < 12; ++i) { mx = max(max(mx, HB[i][0]), HB[i][1]); mx = max(max(mx, HB[i][2]), HB[i][3]); } \
            if (!__any(mx == 0xffffffffu)) break; \
            __builtin_amdgcn_s_sleep(1); \
            _Pragma("unroll") for (int i = 0; i < 12; ++i) GLD4(HB[i], "v", vo16, fsB + (i & 1) * 49152 + (i >> 3) * 4096, ((i >> 1) & 3) * 1024, " sc1"); \
            asm volatile("s_waitcnt vmcnt(0)" : "+v"(HB[0]), "+v"(HB[1]), "+v"(HB[2]), "+v"(HB[3]), "+v"(HB[4]), "+v"(HB[5]), "+v"(HB[6]), "+v"(HB[7]), "+v"(HB[8]), "+v"(HB[9]), "+v"(HB[10]), "+v"(HB[11]) :: "memory"); \
        } } while (0)
#define FINISH(part) do { FINISH1(part, w8); if (w8 < 2) FINISH1(part, 8 + w8); } while (0)
    const char* usrc = ws + WS_UR + (size_t)d * (URS * 2);
    const u32 udst = lb + w * 20480;
    STAMP(0);
    if (kh == 0) {
        u32x4 W0, W1, W2, W3, W4, W5, W6, W7, W8, W9, W10, W11, W12, W13, W14, W15, W16, W17, W18, W19, W20, W21, W22, W23, W24, W25, W26, W27, W28, W29, W30, W31, W32, W33, W34, W35, W36, W37, W38, W39;
        if (cv >= 0) {
        u32 dvw;
        asm volatile("s_load_dword %0, %1, 0x0" : "=s"(dvw) : "s"((const char*)Dv + ((d * 2) & ~3)) : "memory"); SB;
        DMA_M0(udst, 4640); SB;
        DMA_GO1(vo16, usrc + 0, 3072, ""); SB;
        DMA_GO(vo16, usrc + 0, 2048, ""); SB;
        DMA_M0(udst, 0); SB;
        DMA_GO1(vo16, usrc + UR0_OFF + 0, 3072, ""); SB;
        DMA_GO(vo16, usrc + UR0_OFF + 0, 2048, ""); SB;
        asm volatile("s_waitcnt lgkmcnt(0)" : "+s"(dvw) :: "memory"); SB;
        u32x4 AD; { const u32 dv = (d & 1) ? (dvw >> 16) : (dvw & 0xffffu); const int ii = 15 - a - 4 * g;
          AD[0] = ii == 0 ? dv : 0u; AD[1] = ii == 1 ? dv : 0u; AD[2] = ii == 2 ? dv : 0u; AD[3] = ii == 3 ? dv : 0u; } PIN(AD);
        const u32 pA = lb + CV_H + w * CV_HS + (a & 3) * CV_CPY + ((a >> 2) + g) * 16;
        const u32 pB = lb + w * 20480 + (a & 1) * CV_US + (2016 - 32 * (a >> 1) + 8 * g) * 2 - 56 * 64;
        const u32 pS = lb + CV_ST + (16 * a + 4 * g) * 8, pX = lb + w * 20480 + CV_X + (16 * a + 4 * g) * 2;
        const u32 pO = lb + CV_HO + a * 128 + (((w * 4 + g) ^ ((a & 7) << 1)) * 8);
        f32x4 cacc[8];
        __builtin_amdgcn_s_setprio(3); SB;
        STAMP(1);
        u32x4 B1_0_0, B1_0_1, B1_0_2, B1_0_3, B1_0_4, B1_0_5, B1_0_6, B1_0_7, B1_1_0, B1_1_1, B1_1_2, B1_1_3, B1_1_4, B1_1_5, B1_1_6, B1_1_7, B1_2_0, B1_2_1, B1_2_2, B1_2_3, B1_2_4, B1_2_5, B1_2_6, B1_2_7, B1_3_0, B1_3_1, B1_3_2, B1_3_3, B1_3_4, B1_3_5, B1_3_6, B1_3_7, B2_0_0, B2_0_1, B2_0_2, B2_0_3, B2_0_4, B2_0_5, B2_0_6, B2_0_7, B2_1_0, B2_1_1, B2_1_2, B2_1_3, B2_1_4, B2_1_5, B2_1_6, B2_1_7, B2_2_0, B2_2_1, B2_2_2, B2_2_3, B2_2_4, B2_2_5, B2_2_6, B2_2_7, B2_3_0, B2_3_1, B2_3_2, B2_3_3, B2_3_4, B2_3_5, B2_3_6, B2_3_7, B2_4_0, B2_4_1, B2_4_2, B2_4_3, B2_4_4, B2_4_5, B2_4_6, B2_4_7, B2_5_0, B2_5_1, B2_5_2, B2_5_3, B2_5_4, B2_5_5, B2_5_6, B2_5_7, B2_6_0, B2_6_1, B2_6_2, B2_6_3, B2_6_4, B2_6_5, B2_6_6, B2_6_7, B2_7_0, B2_7_1, B2_7_2, B2_7_3, B2_7_4, B2_7_5, B2_7_6, B2_7_7;
        u32x4 A0, A1, A2, A3, A4, A5, A6, A7, A8, A9, A10, A11, A12, A13, A14, A15, A16, A17, A18, A19, A20, A21, A22, A23, A24, A25, A26, A27, A28, A29, A30, A31, A32, A33, A34, A35, A36, A37, A38, A39, A40, A41, A42, A43, A44, A45, A46, A47, A48, A49, A50, A51, A52, A53, A54, A55, A56, A57, A58, A59, A60, A61, A62, A63, A64, A65, A66, A67, A68, A69, A70, A71, A72, A73, A74, A75, A76, A77, A78, A79, A80, A81, A82, A83, A84, A85, A86, A87, A88, A89, A90, A91, A92, A93, A94, A95;
        asm volatile("s_waitcnt vmcnt(0)" ::: "memory"); SB;
        BAR(); SB;
        LRD4(A0, "v", pA, 0); SB;
        LRD4(B1_0_0, "a", pB, 3584); SB;
        LRD4(B1_1_0, "a", pB, 3072); SB;
        LRD4(B1_2_0, "a", pB, 2560); SB;
        LRD4(B1_3_0, "a", pB, 2048); SB;
        LRD4(A1, "v", pA, 64); SB;
        LRD4(B1_0_1, "a", pB, 3648); SB;
        LRD4(B1_1_1, "a", pB, 3136); SB;
        LRD4(B1_2_1, "a", pB, 2624); SB;
        LRD4(B1_3_1, "a", pB, 2112); SB;
        LRD4(A2, "v", pA, 128); SB;
        LRD4(B1_0_2, "a", pB, 3712); SB;
        LRD4(B1_1_2, "a", pB, 3200); SB;
        asm volatile("s_waitcnt lgkmcnt(8)" ::: "memory"); SB;
        MF0(cacc[0], A0, "v", B1_0_0, "a"); SB;
        LRD4(B1_2_2, "a", pB, 2688); SB;
        LRD4(B1_3_2, "a", pB, 2176); SB;
        MF0(cacc[1], A0, "v", B1_1_0, "a"); SB;
        LRD4(A3, "v", pA, 192); SB;
        LRD4(B1_0_3, "a", pB, 3776); SB;
        MF0(cacc[2], A0, "v", B1_2_0, "a"); SB;
        LRD4(B1_1_3, "a", pB, 3264); SB;
        MF0(cacc[3], A0, "v", B1_3_0, "a"); SB;
        MF(cacc[0], AD, "v", B1_0_0, "a"); SB;
        MF(cacc[1], AD, "v", B1_1_0, "a"); SB;
        MF(cacc[2], AD, "v", B1_2_0, "a"); SB;
        MF(cacc[3], AD, "v", B1_3_0, "a"); SB;
        asm volatile("s_waitcnt lgkmcnt(8)" ::: "memory"); SB;
        MF(cacc[0], A1, "v", B1_0_1, "a"); SB;
        LRD4(B1_2_3, "a", pB, 2752); SB;
        LRD4(B1_3_3, "a", pB, 2240); SB;
        MF(cacc[1], A1, "v", B1_1_1, "a"); SB;
        LRD4(A4, "v", pA, 256); SB;
        LRD4(B1_0_4, "a", pB, 3840); SB;
        MF(cacc[2], A1, "v", B1_2_1, "a"); SB;
        LRD4(B1_1_4, "a", pB, 3328); SB;
        MF(cacc[3], A1, "v", B1_3_1, "a"); SB;
        asm volatile("s_waitcnt lgkmcnt(8)" ::: "memory"); SB;
        MF(cacc[0], A2, "v", B1_0_2, "a"); SB;
        LRD4(B1_2_4, "a", pB, 2816); SB;
        LRD4(B1_3_4, "a", pB, 2304); SB;
        MF(cacc[1], A2, "v", B1_1_2, "a"); SB;
        LRD4(A5, "v", pA, 320); SB;
        LRD4(B1_0_5, "a", pB, 3904); SB;
        MF(cacc[2], A2, "v", B1_2_2, "a"); SB;
        LRD4(B1_1_5, "a", pB, 3392); SB;
        MF(cacc[3], A2, "v", B1_3_2, "a"); SB;
        asm volatile("s_waitcnt lgkmcnt(8)" ::: "memory"); SB;
        MF(cacc[0], A3, "v", B1_0_3, "a"); SB;
        LRD4(B1_2_5, "a", pB, 2880); SB;
        LRD4(B1_3_5, "a", pB, 2368); SB;
        MF(cacc[1], A3, "v", B1_1_3, "a"); SB;
        LRD4(A6, "v", pA, 384); SB;
        LRD4(B1_0_6, "a", pB, 3968); SB;
        MF(cacc[2], A3, "v", B1_2_3, "a"); SB;
        LRD4(B1_1_6, "a", pB, 3456); SB;
        MF(cacc[3], A3, "v", B1_3_3, "a"); SB;
        asm volatile("s_waitcnt lgkmcnt(8)" ::: "memory"); SB;
        MF(cacc[0], A4, "v", B1_0_4, "a"); SB;
        LRD4(B1_2_6, "a", pB, 2944); SB;
        LRD4(B1_3_6, "a", pB, 2432); SB;
        MF(cacc[1], A4, "v", B1_1_4, "a"); SB;
        LRD4(A7, "v", pA, 448); SB;
        LRD4(B1_0_7, "v", pB, 4032); SB;
        MF(cacc[2], A4, "v", B1_2_4, "a"); SB;
        LRD4(B1_1_7, "v", pB, 3520); SB;
        MF(cacc[3], A4, "v", B1_3_4, "a"); SB;
        asm volatile("s_waitcnt lgkmcnt(8)" ::: "memory"); SB;
        MF(cacc[0], A5, "v", B1_0_5, "a"); SB;
        LRD4(B1_2_7, "v", pB, 3008); SB;
        LRD4(B1_3_7, "v", pB, 2496); SB;
        MF(cacc[1], A5, "v", B1_1_5, "a"); SB;
        LRD4(A8, "v", pA, 512); SB;
        LRD4(A9, "v", pA, 576); SB;
        MF(cacc[2], A5, "v", B1_2_5, "a"); SB;
        MF(cacc[3], A5, "v", B1_3_5, "a"); SB;
        asm volatile("s_waitcnt lgkmcnt(7)" ::: "memory"); SB;
        MF(cacc[0], A6, "v", B1_0_6, "a"); SB;
        LRD4(A10, "v", pA, 640); SB;
        MF(cacc[1], A6, "v", B1_1_6, "a"); SB;
        MF(cacc[2], A6, "v", B1_2_6, "a"); SB;
        MF(cacc[3], A6, "v", B1_3_6, "a"); SB;
        asm volatile("s_waitcnt lgkmcnt(3)" ::: "memory"); SB;
        MF(cacc[0], A7, "v", B1_0_7, "v"); SB;
        LRD4(A11, "v", pA, 704); SB;
        LRD4(A12, "v", pA, 768); SB;
        MF(cacc[1], A7, "v", B1_1_7, "v"); SB;
        MF(cacc[2], A7, "v", B1_2_7, "v"); SB;
        MF(cacc[3], A7, "v", B1_3_7, "v"); SB;
        STAMP(2);
        asm volatile("s_waitcnt lgkmcnt(4)" ::: "memory"); SB;
        MF(cacc[1], A8, "v", B1_0_0, "a"); SB;
        LRD4(A13, "v", pA, 832); SB;
        MF(cacc[2], A8, "v", B1_1_0, "a"); SB;
        MF(cacc[3], A8, "v", B1_2_0, "a"); SB;
        asm volatile("s_waitcnt lgkmcnt(4)" ::: "memory"); SB;
        MF(cacc[1], A9, "v", B1_0_1, "a"); SB;
        LRD4(A14, "v", pA, 896); SB;
        MF(cacc[2], A9, "v", B1_1_1, "a"); SB;
        MF(cacc[3], A9, "v", B1_2_1, "a"); SB;
        asm volatile("s_waitcnt lgkmcnt(4)" ::: "memory"); SB;
        MF(cacc[1], A10, "v", B1_0_2, "a"); SB;
        LRD4(A15, "v", pA, 960); SB;
        MF(cacc[2], A10, "v", B1_1_2, "a"); SB;
        MF(cacc[3], A10, "v", B1_2_2, "a"); SB;
        asm volatile("s_waitcnt lgkmcnt(4)" ::: "memory"); SB;
        MF(cacc[1], A11, "v", B1_0_3, "a"); SB;
        BAR(); SB;
        LRD4(A16, "v", pA, 1024); SB;
        MF(cacc[2], A11, "v", B1_1_3, "a"); SB;
        MF(cacc[3], A11, "v", B1_2_3, "a"); SB;
        asm volatile("s_waitcnt lgkmcnt(4)" ::: "memory"); SB;
        MF(cacc[1], A12, "v", B1_0_4, "a"); SB;
        LRD4(A17, "v", pA, 1088); SB;
        MF(cacc[2], A12, "v", B1_1_4, "a"); SB;
        MF(cacc[3], A12, "v", B1_2_4, "a"); SB;
        asm volatile("s_waitcnt lgkmcnt(4)" ::: "memory"); SB;
        MF(cacc[1], A13, "v", B1_0_5, "a"); SB;
        LRD4(A18, "v", pA, 1152); SB;
        LRD4(A19, "v", pA, 1216); SB;
        MF(cacc[2], A13, "v", B1_1_5, "a"); SB;
        MF(cacc[3], A13, "v", B1_2_5, "a"); SB;
        asm volatile("s_waitcnt lgkmcnt(5)" ::: "memory"); SB;
        MF(cacc[1], A14, "v", B1_0_6, "a"); SB;
        LRD4(A20, "v", pA, 1280); SB;
        MF(cacc[2], A14, "v", B1_1_6, "a"); SB;
        MF(cacc[3], A14, "v", B1_2_6, "a"); SB;
        f32x4 es0_0, es1_0; u32x2 ex_0, eo_0;
        LRD4(es0_0, "v", pS, 0); SB;
        LRD4(es1_0, "v", pS, 16); SB;
        LRD2(ex_0, pX, 0); SB;
        asm volatile("s_waitcnt lgkmcnt(8)" ::: "memory"); SB;
        MF(cacc[1], A15, "v", B1_0_7, "v"); SB;
        LRD4(A21, "v", pA, 1344); SB;
        LRD4(A22, "v", pA, 1408); SB;
        MF(cacc[2], A15, "v", B1_1_7, "v"); SB;
        MF(cacc[3], A15, "v", B1_2_7, "v"); SB;
        f32x4 es0_1, es1_1; u32x2 ex_1, eo_1;
        LRD4(es0_1, "v", pS, 2048); SB;
        LRD4(es1_1, "v", pS, 2064); SB;
        LRD2(ex_1, pX, 512); SB;
        asm volatile("s_waitcnt lgkmcnt(12)" ::: "memory"); SB;
        MF(cacc[2], A16, "v", B1_0_0, "a"); SB;
        LRD4(A23, "v", pA, 1472); SB;
        MF(cacc[3], A16, "v", B1_1_0, "a"); SB;
        asm volatile("s_waitcnt lgkmcnt(12)" ::: "memory"); SB;
        MF(cacc[2], A17, "v", B1_0_1, "a"); SB;
        LRD4(A24, "v", pA, 1536); SB;
        asm volatile("s_waitcnt lgkmcnt(7)" : "+v"(es0_0), "+v"(es1_0), "+v"(ex_0) :: "memory"); SB;
        float ea_0, eb_0; { float x0 = bflo(ex_0[0]); ea_0 = fmaf(fmaf(x0, es0_0[0], es0_0[1]), cacc[0][0], x0); } PIN(ea_0); SB;
        MF(cacc[3], A17, "v", B1_1_1, "a"); SB;
        { float x1 = bfhi(ex_0[0]); eo_0[0] = pk2(ea_0, fmaf(fmaf(x1, es0_0[2], es0_0[3]), cacc[0][1], x1)); } PIN(eo_0); SB;
        MF(cacc[2], A18, "v", B1_0_2, "a"); SB;
        LRD4(A25, "v", pA, 1600); SB;
        LRD4(A26, "v", pA, 1664); SB;
        MF(cacc[3], A18, "v", B1_1_2, "a"); SB;
        { float x2 = bflo(ex_0[1]); eb_0 = fmaf(fmaf(x2, es1_0[0], es1_0[1]), cacc[0][2], x2); } PIN(eb_0); SB;
        MF(cacc[2], A19, "v", B1_0_3, "a"); SB;
        LRD4(A27, "v", pA, 1728); SB;
        LRD4(A28, "v", pA, 1792); SB;
        MF(cacc[3], A19, "v", B1_1_3, "a"); SB;
        { float x3 = bfhi(ex_0[1]); eo_0[1] = pk2(eb_0, fmaf(fmaf(x3, es1_0[2], es1_0[3]), cacc[0][3], x3)); } SB;
        LWR2(pO, eo_0, 0); SB;
        MF(cacc[2], A20, "v", B1_0_4, "a"); SB;
        LRD4(A29, "v", pA, 1856); SB;
        asm volatile("s_waitcnt lgkmcnt(8)" : "+v"(es0_1), "+v"(es1_1), "+v"(ex_1) :: "memory"); SB;
        float ea_1, eb_1; { float x0 = bflo(ex_1[0]); ea_1 = fmaf(fmaf(x0, es0_1[0], es0_1[1]), cacc[1][0], x0); } PIN(ea_1); SB;
        MF(cacc[3], A20, "v", B1_1_4, "a"); SB;
        LRD4(A30, "v", pA, 1920); SB;
        { float x1 = bfhi(ex_1[0]); eo_1[0] = pk2(ea_1, fmaf(fmaf(x1, es0_1[2], es0_1[3]), cacc[1][1], x1)); } PIN(eo_1); SB;
        MF(cacc[2], A21, "v", B1_0_5, "a"); SB;
        LRD4(A31, "v", pA, 1984); SB;
        LRD4(A32, "v", pA, 0); SB;
        MF(cacc[3], A21, "v", B1_1_5, "a"); SB;
        BAR(); SB;
        LRD4(B2_4_0, "a", pB, 1536); SB;
        LRD4(B2_5_0, "a", pB, 1024); SB;
        MF(cacc[2], A22, "v", B1_0_6, "a"); SB;
        { float x2 = bflo(ex_1[1]); eb_1 = fmaf(fmaf(x2, es1_1[0], es1_1[1]), cacc[1][2], x2); } PIN(eb_1); SB;
        MF(cacc[3], A22, "v", B1_1_6, "a"); SB;
        { float x3 = bfhi(ex_1[1]); eo_1[1] = pk2(eb_1, fmaf(fmaf(x3, es1_1[2], es1_1[3]), cacc[1][3], x3)); } SB;
        LWR2(pO, eo_1, 2048); SB;
        f32x4 es0_2, es1_2; u32x2 ex_2, eo_2;
        LRD4(es0_2, "v", pS, 20480); SB;
        LRD4(es1_2, "v", pS, 20496); SB;
        LRD2(ex_2, pX, 1024); SB;
        asm volatile("s_waitcnt lgkmcnt(15)" ::: "memory"); SB;
        MF(cacc[2], A23, "v", B1_0_7, "v"); SB;
        MF(cacc[3], A23, "v", B1_1_7, "v"); SB;
        MF(cacc[3], A24, "v", B1_0_0, "a"); SB;
        asm volatile("s_waitcnt lgkmcnt(14)" ::: "memory"); SB;
        MF(cacc[3], A25, "v", B1_0_1, "a"); SB;
        asm volatile("s_waitcnt lgkmcnt(0)" : "+v"(es0_2), "+v"(es1_2), "+v"(ex_2) :: "memory"); SB;
        float ea_2, eb_2; { float x0 = bflo(ex_2[0]); ea_2 = fmaf(fmaf(x0, es0_2[0], es0_2[1]), cacc[2][0], x0); } PIN(ea_2); SB;
        MF(cacc[3], A26, "v", B1_0_2, "a"); SB;
        LRD4(B2_6_0, "a", pB, 512); SB;
        LRD4(B2_7_0, "a", pB, 0); SB;
        MF(cacc[3], A27, "v", B1_0_3, "a"); SB;
        LRD4(A33, "v", pA, 64); SB;
        LRD4(B2_4_1, "a", pB, 1600); SB;
        MF(cacc[3], A28, "v", B1_0_4, "a"); SB;
        LRD4(B2_5_1, "a", pB, 1088); SB;
        LRD4(B2_6_1, "a", pB, 576); SB;
        MF(cacc[3], A29, "v", B1_0_5, "a"); SB;
        LRD4(B2_7_1, "a", pB, 64); SB;
        { float x1 = bfhi(ex_2[0]); eo_2[0] = pk2(ea_2, fmaf(fmaf(x1, es0_2[2], es0_2[3]), cacc[2][1], x1)); } PIN(eo_2); SB;
        MF(cacc[3], A30, "v", B1_0_6, "a"); SB;
        { float x2 = bflo(ex_2[1]); eb_2 = fmaf(fmaf(x2, es1_2[0], es1_2[1]), cacc[2][2], x2); } PIN(eb_2); SB;
        f32x4 es0_3, es1_3; u32x2 ex_3, eo_3;
        LRD4(es0_3, "v", pS, 22528); SB;
        LRD4(es1_3, "v", pS, 22544); SB;
        LRD2(ex_3, pX, 1536); SB;
        MF(cacc[3], A31, "v", B1_0_7, "v"); SB;
        LRD4(A34, "v", pA, 128); SB;
        LRD4(B2_4_2, "a", pB, 1664); SB;
        STAMP(3);
        asm volatile("s_waitcnt lgkmcnt(10)" ::: "memory"); SB;
        MF0(cacc[4], A32, "v", B2_4_0, "a"); SB;
        LRD4(B2_5_2, "a", pB, 1152); SB;
        LRD4(B2_6_2, "a", pB, 640); SB;
        { float x3 = bfhi(ex_2[1]); eo_2[1] = pk2(eb_2, fmaf(fmaf(x3, es1_2[2], es1_2[3]), cacc[2][3], x3)); } SB;
        LWR2(pO, eo_2, 4096); SB;
        MF0(cacc[5], A32, "v", B2_5_0, "a"); SB;
        MF0(cacc[6], A32, "v", B2_6_0, "a"); SB;
        MF0(cacc[7], A32, "v", B2_7_0, "a"); SB;
        MF(cacc[4], AD, "v", B2_4_0, "a"); SB;
        MF(cacc[5], AD, "v", B2_5_0, "a"); SB;
        MF(cacc[6], AD, "v", B2_6_0, "a"); SB;
        MF(cacc[7], AD, "v", B2_7_0, "a"); SB;
        asm volatile("s_waitcnt lgkmcnt(8)" ::: "memory"); SB;
        MF(cacc[4], A33, "v", B2_4_1, "a"); SB;
        LRD4(B2_7_2, "a", pB, 128); SB;
        LRD4(A35, "v", pA, 192); SB;
        asm volatile("s_waitcnt lgkmcnt(7)" : "+v"(es0_3), "+v"(es1_3), "+v"(ex_3) :: "memory"); SB;
        float ea_3, eb_3; { float x0 = bflo(ex_3[0]); ea_3 = fmaf(fmaf(x0, es0_3[0], es0_3[1]), cacc[3][0], x0); } PIN(ea_3); SB;
        MF(cacc[5], A33, "v", B2_5_1, "a"); SB;
        LRD4(B2_4_3, "a", pB, 1728); SB;
        LRD4(B2_5_3, "a", pB, 1216); SB;
        { float x1 = bfhi(ex_3[0]); eo_3[0] = pk2(ea_3, fmaf(fmaf(x1, es0_3[2], es0_3[3]), cacc[3][1], x1)); } PIN(eo_3); SB;
        MF(cacc[6], A33, "v", B2_6_1, "a"); SB;
        LRD4(B2_6_3, "a", pB, 704); SB;
        LRD4(B2_7_3, "a", pB, 192); SB;
        { float x2 = bflo(ex_3[1]); eb_3 = fmaf(fmaf(x2, es1_3[0], es1_3[1]), cacc[3][2], x2); } PIN(eb_3); SB;
        MF(cacc[7], A33, "v", B2_7_1, "a"); SB;
        LRD4(A36, "v", pA, 256); SB;
        LRD4(B2_4_4, "a", pB, 1792); SB;
        { float x3 = bfhi(ex_3[1]); eo_3[1] = pk2(eb_3, fmaf(fmaf(x3, es1_3[2], es1_3[3]), cacc[3][3], x3)); } SB;
        LWR2(pO, eo_3, 6144); SB;
        asm volatile("s_waitcnt lgkmcnt(8)" ::: "memory"); SB;
        MF(cacc[4], A34, "v", B2_4_2, "a"); SB;
        LRD4(B2_5_4, "a", pB, 1280); SB;
        LRD4(B2_6_4, "a", pB, 768); SB;
        MF(cacc[5], A34, "v", B2_5_2, "a"); SB;
        LRD4(B2_7_4, "a", pB, 256); SB;
        LRD4(A37, "v", pA, 320); SB;
        MF(cacc[6], A34, "v", B2_6_2, "a"); SB;
        LRD4(B2_4_5, "a", pB, 1856); SB;
        MF(cacc[7], A34, "v", B2_7_2, "a"); SB;
        asm volatile("s_waitcnt lgkmcnt(8)" ::: "memory"); SB;
        MF(cacc[4], A35, "v", B2_4_3, "a"); SB;
        LRD4(B2_5_5, "a", pB, 1344); SB;
        LRD4(B2_6_5, "a", pB, 832); SB;
        MF(cacc[5], A35, "v", B2_5_3, "a"); SB;
        LRD4(B2_7_5, "a", pB, 320); SB;
        LRD4(A38, "v", pA, 384); SB;
        MF(cacc[6], A35, "v", B2_6_3, "a"); SB;
        LRD4(B2_4_6, "a", pB, 1920); SB;
        MF(cacc[7], A35, "v", B2_7_3, "a"); SB;
        asm volatile("s_waitcnt lgkmcnt(10)" ::: "memory"); SB;
        BAR(); SB;
        asm volatile("s_waitcnt lgkmcnt(7)" ::: "memory"); SB;
        MF(cacc[4], A36, "v", B2_4_4, "a"); SB;
        LRD4(B2_5_6, "a", pB, 1408); SB;
        LRD4(B2_6_6, "a", pB, 896); SB;
        MF(cacc[5], A36, "v", B2_5_4, "a"); SB;
        LRD4(B2_7_6, "a", pB, 384); SB;
        LRD4(A39, "v", pA, 448); SB;
        MF(cacc[6], A36, "v", B2_6_4, "a"); SB;
        LRD4(B2_4_7, "v", pB, 1984); SB;
        LRD4(B2_5_7, "v", pB, 1472); SB;
        MF(cacc[7], A36, "v", B2_7_4, "a"); SB;
        asm volatile("s_waitcnt lgkmcnt(8)" ::: "memory"); SB;
        MF(cacc[4], A37, "v", B2_4_5, "a"); SB;
        LRD4(B2_6_7, "v", pB, 960); SB;
        LRD4(B2_7_7, "v", pB, 448); SB;
        MF(cacc[5], A37, "v", B2_5_5, "a"); SB;
        LRD4(A40, "v", pA, 512); SB;
        LRD4(B2_3_0, "a", pB, 2048); SB;
        MF(cacc[6], A37, "v", B2_6_5, "a"); SB;
        LRD4(A41, "v", pA, 576); SB;
        MF(cacc[7], A37, "v", B2_7_5, "a"); SB;
        asm volatile("s_waitcnt lgkmcnt(8)" ::: "memory"); SB;
        MF(cacc[4], A38, "v", B2_4_6, "a"); SB;
        LRD4(B2_3_1, "a", pB, 2112); SB;
        LRD4(A42, "v", pA, 640); SB;
        MF(cacc[5], A38, "v", B2_5_6, "a"); SB;
        LRD4(B2_3_2, "a", pB, 2176); SB;
        MF(cacc[6], A38, "v", B2_6_6, "a"); SB;
        MF(cacc[7], A38, "v", B2_7_6, "a"); SB;
        asm volatile("s_waitcnt lgkmcnt(6)" ::: "memory"); SB;
        MF(cacc[4], A39, "v", B2_4_7, "v"); SB;
        LRD4(A43, "v", pA, 704); SB;
        LRD4(B2_3_3, "a", pB, 2240); SB;
        MF(cacc[5], A39, "v", B2_5_7, "v"); SB;
        MF(cacc[6], A39, "v", B2_6_7, "v"); SB;
        MF(cacc[7], A39, "v", B2_7_7, "v"); SB;
        asm volatile("s_waitcnt lgkmcnt(6)" ::: "memory"); SB;
        MF(cacc[4], A40, "v", B2_3_0, "a"); SB;
        LRD4(A44, "v", pA, 768); SB;
        LRD4(B2_3_4, "a", pB, 2304); SB;
        MF(cacc[5], A40, "v", B2_4_0, "a"); SB;
        MF(cacc[6], A40, "v", B2_5_0, "a"); SB;
        MF(cacc[7], A40, "v", B2_6_0, "a"); SB;
        asm volatile("s_waitcnt lgkmcnt(6)" ::: "memory"); SB;
        MF(cacc[4], A41, "v", B2_3_1, "a"); SB;
        LRD4(A45, "v", pA, 832); SB;
        LRD4(B2_3_5, "a", pB, 2368); SB;
        MF(cacc[5], A41, "v", B2_4_1, "a"); SB;
        MF(cacc[6], A41, "v", B2_5_1, "a"); SB;
        MF(cacc[7], A41, "v", B2_6_1, "a"); SB;
        asm volatile("s_waitcnt lgkmcnt(6)" ::: "memory"); SB;
        MF(cacc[4], A42, "v", B2_3_2, "a"); SB;
        LRD4(A46, "v", pA, 896); SB;
        LRD4(B2_3_6, "a", pB, 2432); SB;
        MF(cacc[5], A42, "v", B2_4_2, "a"); SB;
        MF(cacc[6], A42, "v", B2_5_2, "a"); SB;
        MF(cacc[7], A42, "v", B2_6_2, "a"); SB;
        asm volatile("s_waitcnt lgkmcnt(6)" ::: "memory"); SB;
        MF(cacc[4], A43, "v", B2_3_3, "a"); SB;
        LRD4(A47, "v", pA, 960); SB;
        LRD4(B2_3_7, "v", pB, 2496); SB;
        MF(cacc[5], A43, "v", B2_4_3, "a"); SB;
        MF(cacc[6], A43, "v", B2_5_3, "a"); SB;
        MF(cacc[7], A43, "v", B2_6_3, "a"); SB;
        asm volatile("s_waitcnt lgkmcnt(6)" ::: "memory"); SB;
        MF(cacc[4], A44, "v", B2_3_4, "a"); SB;
        LRD4(A48, "v", pA, 1024); SB;
        LRD4(B2_2_0, "a", pB, 2560); SB;
        MF(cacc[5], A44, "v", B2_4_4, "a"); SB;
        MF(cacc[6], A44, "v", B2_5_4, "a"); SB;
        MF(cacc[7], A44, "v", B2_6_4, "a"); SB;
        asm volatile("s_waitcnt lgkmcnt(6)" ::: "memory"); SB;
        MF(cacc[4], A45, "v", B2_3_5, "a"); SB;
        LRD4(A49, "v", pA, 1088); SB;
        LRD4(B2_2_1, "a", pB, 2624); SB;
        MF(cacc[5], A45, "v", B2_4_5, "a"); SB;
        MF(cacc[6], A45, "v", B2_5_5, "a"); SB;
        MF(cacc[7], A45, "v", B2_6_5, "a"); SB;
        asm volatile("s_waitcnt lgkmcnt(6)" ::: "memory"); SB;
        MF(cacc[4], A46, "v", B2_3_6, "a"); SB;
        LRD4(A50, "v", pA, 1152); SB;
        LRD4(B2_2_2, "a", pB, 2688); SB;
        MF(cacc[5], A46, "v", B2_4_6, "a"); SB;
        MF(cacc[6], A46, "v", B2_5_6, "a"); SB;
        MF(cacc[7], A46, "v", B2_6_6, "a"); SB;
        asm volatile("s_waitcnt lgkmcnt(6)" ::: "memory"); SB;
        MF(cacc[4], A47, "v", B2_3_7, "v"); SB;
        LRD4(A51, "v", pA, 1216); SB;
        LRD4(B2_2_3, "a", pB, 2752); SB;
        MF(cacc[5], A47, "v", B2_4_7, "v"); SB;
        MF(cacc[6], A47, "v", B2_5_7, "v"); SB;
        MF(cacc[7], A47, "v", B2_6_7, "v"); SB;
        asm volatile("s_waitcnt lgkmcnt(6)" ::: "memory"); SB;
        MF(cacc[4], A48, "v", B2_2_0, "a"); SB;
        LRD4(A52, "v", pA, 1280); SB;
        LRD4(B2_2_4, "a", pB, 2816); SB;
        MF(cacc[5], A48, "v", B2_3_0, "a"); SB;
        MF(cacc[6], A48, "v", B2_4_0, "a"); SB;
        MF(cacc[7], A48, "v", B2_5_0, "a"); SB;
        asm volatile("s_waitcnt lgkmcnt(6)" ::: "memory"); SB;
        MF(cacc[4], A49, "v", B2_2_1, "a"); SB;
        LRD4(A53, "v", pA, 1344); SB;
        LRD4(B2_2_5, "a", pB, 2880); SB;
        MF(cacc[5], A49, "v", B2_3_1, "a"); SB;
        MF(cacc[6], A49, "v", B2_4_1, "a"); SB;
        MF(cacc[7], A49, "v", B2_5_1, "a"); SB;
        asm volatile("s_waitcnt lgkmcnt(6)" ::: "memory"); SB;
        MF(cacc[4], A50, "v", B2_2_2, "a"); SB;
        LRD4(A54, "v", pA, 1408); SB;
        LRD4(B2_2_6, "a", pB, 2944); SB;
        MF(cacc[5], A50, "v", B2_3_2, "a"); SB;
        MF(cacc[6], A50, "v", B2_4_2, "a"); SB;
        MF(cacc[7], A50, "v", B2_5_2, "a"); SB;
        asm volatile("s_waitcnt lgkmcnt(6)" ::: "memory"); SB;
        MF(cacc[4], A51, "v", B2_2_3, "a"); SB;
        LRD4(A55, "v", pA, 1472); SB;
        LRD4(B2_2_7, "v", pB, 3008); SB;
        MF(cacc[5], A51, "v", B2_3_3, "a"); SB;
        MF(cacc[6], A51, "v", B2_4_3, "a"); SB;
        MF(cacc[7], A51, "v", B2_5_3, "a"); SB;
        asm volatile("s_waitcnt lgkmcnt(6)" ::: "memory"); SB;
        MF(cacc[4], A52, "v", B2_2_4, "a"); SB;
        LRD4(A56, "v", pA, 1536); SB;
        LRD4(B2_1_0, "a", pB, 3072); SB;
        MF(cacc[5], A52, "v", B2_3_4, "a"); SB;
        MF(cacc[6], A52, "v", B2_4_4, "a"); SB;
        MF(cacc[7], A52, "v", B2_5_4, "a"); SB;
        asm volatile("s_waitcnt lgkmcnt(6)" ::: "memory"); SB;
        MF(cacc[4], A53, "v", B2_2_5, "a"); SB;
        LRD4(A57, "v", pA, 1600); SB;
        LRD4(B2_1_1, "a", pB, 3136); SB;
        MF(cacc[5], A53, "v", B2_3_5, "a"); SB;
        MF(cacc[6], A53, "v", B2_4_5, "a"); SB;
        MF(cacc[7], A53, "v", B2_5_5, "a"); SB;
        asm volatile("s_waitcnt lgkmcnt(6)" ::: "memory"); SB;
        MF(cacc[4], A54, "v", B2_2_6, "a"); SB;
        LRD4(A58, "v", pA, 1664); SB;
        LRD4(B2_1_2, "a", pB, 3200); SB;
        MF(cacc[5], A54, "v", B2_3_6, "a"); SB;
        MF(cacc[6], A54, "v", B2_4_6, "a"); SB;
        MF(cacc[7], A54, "v", B2_5_6, "a"); SB;
        asm volatile("s_waitcnt lgkmcnt(6)" ::: "memory"); SB;
        MF(cacc[4], A55, "v", B2_2_7, "v"); SB;
        LRD4(A59, "v", pA, 1728); SB;
        LRD4(B2_1_3, "a", pB, 3264); SB;
        MF(cacc[5], A55, "v", B2_3_7, "v"); SB;
        MF(cacc[6], A55, "v", B2_4_7, "v"); SB;
        MF(cacc[7], A55, "v", B2_5_7, "v"); SB;
        asm volatile("s_waitcnt lgkmcnt(6)" ::: "memory"); SB;
        MF(cacc[4], A56, "v", B2_1_0, "a"); SB;
        LRD4(A60, "v", pA, 1792); SB;
        LRD4(B2_1_4, "a", pB, 3328); SB;
        MF(cacc[5], A56, "v", B2_2_0, "a"); SB;
        MF(cacc[6], A56, "v", B2_3_0, "a"); SB;
        MF(cacc[7], A56, "v", B2_4_0, "a"); SB;
        asm volatile("s_waitcnt lgkmcnt(6)" ::: "memory"); SB;
        MF(cacc[4], A57, "v", B2_1_1, "a"); SB;
        LRD4(A61, "v", pA, 1856); SB;
        LRD4(B2_1_5, "a", pB, 3392); SB;
        MF(cacc[5], A57, "v", B2_2_1, "a"); SB;
        MF(cacc[6], A57, "v", B2_3_1, "a"); SB;
        MF(cacc[7], A57, "v", B2_4_1, "a"); SB;
        asm volatile("s_waitcnt lgkmcnt(6)" ::: "memory"); SB;
        MF(cacc[4], A58, "v", B2_1_2, "a"); SB;
        LRD4(A62, "v", pA, 1920); SB;
        LRD4(B2_1_6, "a", pB, 3456); SB;
        MF(cacc[5], A58, "v", B2_2_2, "a"); SB;
        MF(cacc[6], A58, "v", B2_3_2, "a"); SB;
        MF(cacc[7], A58, "v", B2_4_2, "a"); SB;
        asm volatile("s_waitcnt lgkmcnt(6)" ::: "memory"); SB;
        MF(cacc[4], A59, "v", B2_1_3, "a"); SB;
        LRD4(A63, "v", pA, 1984); SB;
        LRD4(B2_1_7, "v", pB, 3520); SB;
        MF(cacc[5], A59, "v", B2_2_3, "a"); SB;
        MF(cacc[6], A59, "v", B2_3_3, "a"); SB;
        MF(cacc[7], A59, "v", B2_4_3, "a"); SB;
        asm volatile("s_waitcnt lgkmcnt(6)" ::: "memory"); SB;
        MF(cacc[4], A60, "v", B2_1_4, "a"); SB;
        BAR(); SB;
        LRD4(A64, "v", pA, 2048); SB;
        LRD4(B2_0_0, "a", pB, 3584); SB;
        MF(cacc[5], A60, "v", B2_2_4, "a"); SB;
        MF(cacc[6], A60, "v", B2_3_4, "a"); SB;
        MF(cacc[7], A60, "v", B2_4_4, "a"); SB;
        asm volatile("s_waitcnt lgkmcnt(6)" ::: "memory"); SB;
        MF(cacc[4], A61, "v", B2_1_5, "a"); SB;
        LRD4(A65, "v", pA, 2112); SB;
        LRD4(B2_0_1, "a", pB, 3648); SB;
        MF(cacc[5], A61, "v", B2_2_5, "a"); SB;
        MF(cacc[6], A61, "v", B2_3_5, "a"); SB;
        MF(cacc[7], A61, "v", B2_4_5, "a"); SB;
        asm volatile("s_waitcnt lgkmcnt(6)" ::: "memory"); SB;
        MF(cacc[4], A62, "v", B2_1_6, "a"); SB;
        LRD4(A66, "v", pA, 2176); SB;
        LRD4(B2_0_2, "a", pB, 3712); SB;
        MF(cacc[5], A62, "v", B2_2_6, "a"); SB;
        MF(cacc[6], A62, "v", B2_3_6, "a"); SB;
        MF(cacc[7], A62, "v", B2_4_6, "a"); SB;
        asm volatile("s_waitcnt lgkmcnt(6)" ::: "memory"); SB;
        MF(cacc[4], A63, "v", B2_1_7, "v"); SB;
        LRD4(A67, "v", pA, 2240); SB;
        LRD4(B2_0_3, "a", pB, 3776); SB;
        MF(cacc[5], A63, "v", B2_2_7, "v"); SB;
        MF(cacc[6], A63, "v", B2_3_7, "v"); SB;
        MF(cacc[7], A63, "v", B2_4_7, "v"); SB;
        STAMP(4);
        asm volatile("s_waitcnt lgkmcnt(6)" ::: "memory"); SB;
        MF(cacc[4], A64, "v", B2_0_0, "a"); SB;
        LRD4(A68, "v", pA, 2304); SB;
        LRD4(B2_0_4, "a", pB, 3840); SB;
        MF(cacc[5], A64, "v", B2_1_0, "a"); SB;
        MF(cacc[6], A64, "v", B2_2_0, "a"); SB;
        MF(cacc[7], A64, "v", B2_3_0, "a"); SB;
        asm volatile("s_waitcnt lgkmcnt(6)" ::: "memory"); SB;
        MF(cacc[4], A65, "v", B2_0_1, "a"); SB;
        LRD4(A69, "v", pA, 2368); SB;
        LRD4(B2_0_5, "a", pB, 3904); SB;
        MF(cacc[5], A65, "v", B2_1_1, "a"); SB;
        MF(cacc[6], A65, "v", B2_2_1, "a"); SB;
        MF(cacc[7], A65, "v", B2_3_1, "a"); SB;
        asm volatile("s_waitcnt lgkmcnt(6)" ::: "memory"); SB;
        MF(cacc[4], A66, "v", B2_0_2, "a"); SB;
        LRD4(A70, "v", pA, 2432); SB;
        LRD4(B2_0_6, "a", pB, 3968); SB;
        MF(cacc[5], A66, "v", B2_1_2, "a"); SB;
        MF(cacc[6], A66, "v", B2_2_2, "a"); SB;
        MF(cacc[7], A66, "v", B2_3_2, "a"); SB;
        asm volatile("s_waitcnt lgkmcnt(6)" ::: "memory"); SB;
        MF(cacc[4], A67, "v", B2_0_3, "a"); SB;
        LRD4(A71, "v", pA, 2496); SB;
        LRD4(B2_0_7, "v", pB, 4032); SB;
        MF(cacc[5], A67, "v", B2_1_3, "a"); SB;
        MF(cacc[6], A67, "v", B2_2_3, "a"); SB;
        MF(cacc[7], A67, "v", B2_3_3, "a"); SB;
        asm volatile("s_waitcnt lgkmcnt(6)" ::: "memory"); SB;
        MF(cacc[4], A68, "v", B2_0_4, "a"); SB;
        LRD4(A72, "v", pA, 2560); SB;
        MF(cacc[5], A68, "v", B2_1_4, "a"); SB;
        MF(cacc[6], A68, "v", B2_2_4, "a"); SB;
        MF(cacc[7], A68, "v", B2_3_4, "a"); SB;
        asm volatile("s_waitcnt lgkmcnt(5)" ::: "memory"); SB;
        MF(cacc[4], A69, "v", B2_0_5, "a"); SB;
        LRD4(A73, "v", pA, 2624); SB;
        MF(cacc[5], A69, "v", B2_1_5, "a"); SB;
        MF(cacc[6], A69, "v", B2_2_5, "a"); SB;
        MF(cacc[7], A69, "v", B2_3_5, "a"); SB;
        asm volatile("s_waitcnt lgkmcnt(4)" ::: "memory"); SB;
        MF(cacc[4], A70, "v", B2_0_6, "a"); SB;
        LRD4(A74, "v", pA, 2688); SB;
        MF(cacc[5], A70, "v", B2_1_6, "a"); SB;
        MF(cacc[6], A70, "v", B2_2_6, "a"); SB;
        MF(cacc[7], A70, "v", B2_3_6, "a"); SB;
        f32x4 es0_4, es1_4; u32x2 ex_4, eo_4;
        LRD4(es0_4, "v", pS, 40960); SB;
        LRD4(es1_4, "v", pS, 40976); SB;
        LRD2(ex_4, pX, 2048); SB;
        asm volatile("s_waitcnt lgkmcnt(6)" ::: "memory"); SB;
        MF(cacc[4], A71, "v", B2_0_7, "v"); SB;
        LRD4(A75, "v", pA, 2752); SB;
        LRD4(A76, "v", pA, 2816); SB;
        MF(cacc[5], A71, "v", B2_1_7, "v"); SB;
        MF(cacc[6], A71, "v", B2_2_7, "v"); SB;
        MF(cacc[7], A71, "v", B2_3_7, "v"); SB;
        asm volatile("s_waitcnt lgkmcnt(7)" ::: "memory"); SB;
        MF(cacc[5], A72, "v", B2_0_0, "a"); SB;
        LRD4(A77, "v", pA, 2880); SB;
        MF(cacc[6], A72, "v", B2_1_0, "a"); SB;
        MF(cacc[7], A72, "v", B2_2_0, "a"); SB;
        BAR(); SB;
        asm volatile("s_waitcnt lgkmcnt(7)" ::: "memory"); SB;
        MF(cacc[5], A73, "v", B2_0_1, "a"); SB;
        LRD4(A78, "v", pA, 2944); SB;
        asm volatile("s_waitcnt lgkmcnt(4)" : "+v"(es0_4), "+v"(es1_4), "+v"(ex_4) :: "memory"); SB;
        float ea_4, eb_4; { float x0 = bflo(ex_4[0]); ea_4 = fmaf(fmaf(x0, es0_4[0], es0_4[1]), cacc[4][0], x0); } PIN(ea_4); SB;
        MF(cacc[6], A73, "v", B2_1_1, "a"); SB;
        { float x1 = bfhi(ex_4[0]); eo_4[0] = pk2(ea_4, fmaf(fmaf(x1, es0_4[2], es0_4[3]), cacc[4][1], x1)); } PIN(eo_4); SB;
        MF(cacc[7], A73, "v", B2_2_1, "a"); SB;
        { float x2 = bflo(ex_4[1]); eb_4 = fmaf(fmaf(x2, es1_4[0], es1_4[1]), cacc[4][2], x2); } PIN(eb_4); SB;
        MF(cacc[5], A74, "v", B2_0_2, "a"); SB;
        LRD4(A79, "v", pA, 3008); SB;
        { float x3 = bfhi(ex_4[1]); eo_4[1] = pk2(eb_4, fmaf(fmaf(x3, es1_4[2], es1_4[3]), cacc[4][3], x3)); } SB;
        LWR2(pO, eo_4, 0); SB;
        MF(cacc[6], A74, "v", B2_1_2, "a"); SB;
        MF(cacc[7], A74, "v", B2_2_2, "a"); SB;
        asm volatile("s_waitcnt lgkmcnt(5)" ::: "memory"); SB;
        MF(cacc[5], A75, "v", B2_0_3, "a"); SB;
        LRD4(A80, "v", pA, 3072); SB;
        MF(cacc[6], A75, "v", B2_1_3, "a"); SB;
        MF(cacc[7], A75, "v", B2_2_3, "a"); SB;
        asm volatile("s_waitcnt lgkmcnt(5)" ::: "memory"); SB;
        MF(cacc[5], A76, "v", B2_0_4, "a"); SB;
        LRD4(A81, "v", pA, 3136); SB;
        MF(cacc[6], A76, "v", B2_1_4, "a"); SB;
        MF(cacc[7], A76, "v", B2_2_4, "a"); SB;
        GLD4(W0, "v", vo16, wsrc + 0, 0, ""); SB;
        asm volatile("s_waitcnt lgkmcnt(5)" ::: "memory"); SB;
        MF(cacc[5], A77, "v", B2_0_5, "a"); SB;
        LRD4(A82, "v", pA, 3200); SB;
        LRD4(A83, "v", pA, 3264); SB;
        MF(cacc[6], A77, "v", B2_1_5, "a"); SB;
        MF(cacc[7], A77, "v", B2_2_5, "a"); SB;
        asm volatile("s_waitcnt lgkmcnt(6)" ::: "memory"); SB;
        MF(cacc[5], A78, "v", B2_0_6, "a"); SB;
        LRD4(A84, "v", pA, 3328); SB;
        MF(cacc[6], A78, "v", B2_1_6, "a"); SB;
        MF(cacc[7], A78, "v", B2_2_6, "a"); SB;
        f32x4 es0_5, es1_5; u32x2 ex_5, eo_5;
        LRD4(es0_5, "v", pS, 43008); SB;
        LRD4(es1_5, "v", pS, 43024); SB;
        LRD2(ex_5, pX, 2560); SB;
        GLD4(W1, "v", vo16, wsrc + 0, 1024, ""); SB;
        asm volatile("s_waitcnt lgkmcnt(9)" ::: "memory"); SB;
        MF(cacc[5], A79, "v", B2_0_7, "v"); SB;
        LRD4(A85, "v", pA, 3392); SB;
        LRD4(A86, "v", pA, 3456); SB;
        MF(cacc[6], A79, "v", B2_1_7, "v"); SB;
        MF(cacc[7], A79, "v", B2_2_7, "v"); SB;
        asm volatile("s_waitcnt lgkmcnt(9)" ::: "memory"); SB;
        MF(cacc[6], A80, "v", B2_0_0, "a"); SB;
        LRD4(A87, "v", pA, 3520); SB;
        MF(cacc[7], A80, "v", B2_1_0, "a"); SB;
        GLD4(W2, "v", vo16, wsrc + 0, 3072, ""); SB;
        asm volatile("s_waitcnt lgkmcnt(9)" ::: "memory"); SB;
        MF(cacc[6], A81, "v", B2_0_1, "a"); SB;
        LRD4(A88, "v", pA, 3584); SB;
        asm volatile("s_waitcnt lgkmcnt(4)" : "+v"(es0_5), "+v"(es1_5), "+v"(ex_5) :: "memory"); SB;
        float ea_5, eb_5; { float x0 = bflo(ex_5[0]); ea_5 = fmaf(fmaf(x0, es0_5[0], es0_5[1]), cacc[5][0], x0); } PIN(ea_5); SB;
        MF(cacc[7], A81, "v", B2_1_1, "a"); SB;
        { float x1 = bfhi(ex_5[0]); eo_5[0] = pk2(ea_5, fmaf(fmaf(x1, es0_5[2], es0_5[3]), cacc[5][1], x1)); } PIN(eo_5); SB;
        MF(cacc[6], A82, "v", B2_0_2, "a"); SB;
        LRD4(A89, "v", pA, 3648); SB;
        LRD4(A90, "v", pA, 3712); SB;
        MF(cacc[7], A82, "v", B2_1_2, "a"); SB;
        { float x2 = bflo(ex_5[1]); eb_5 = fmaf(fmaf(x2, es1_5[0], es1_5[1]), cacc[5][2], x2); } PIN(eb_5); SB;
        GLD4(W3, "v", vo16, wsrc + 4096, 0, ""); SB;
        MF(cacc[6], A83, "v", B2_0_3, "a"); SB;
        LRD4(A91, "v", pA, 3776); SB;
        LRD4(A92, "v", pA, 3840); SB;
        MF(cacc[7], A83, "v", B2_1_3, "a"); SB;
        { float x3 = bfhi(ex_5[1]); eo_5[1] = pk2(eb_5, fmaf(fmaf(x3, es1_5[2], es1_5[3]), cacc[5][3], x3)); } SB;
        LWR2(pO, eo_5, 2048); SB;
        MF(cacc[6], A84, "v", B2_0_4, "a"); SB;
        LRD4(A93, "v", pA, 3904); SB;
        LRD4(A94, "v", pA, 3968); SB;
        MF(cacc[7], A84, "v", B2_1_4, "a"); SB;
        GLD4(W4, "v", vo16, wsrc + 4096, 2048, ""); SB;
        asm volatile("s_waitcnt lgkmcnt(10)" ::: "memory"); SB;
        MF(cacc[6], A85, "v", B2_0_5, "a"); SB;
        LRD4(A95, "v", pA, 4032); SB;
        MF(cacc[7], A85, "v", B2_1_5, "a"); SB;
        asm volatile("s_waitcnt lgkmcnt(10)" ::: "memory"); SB;
        MF(cacc[6], A86, "v", B2_0_6, "a"); SB;
        MF(cacc[7], A86, "v", B2_1_6, "a"); SB;
        f32x4 es0_6, es1_6; u32x2 ex_6, eo_6;
        LRD4(es0_6, "v", pS, 61440); SB;
        LRD4(es1_6, "v", pS, 61456); SB;
        LRD2(ex_6, pX, 3072); SB;
        GLD4(W5, "v", vo16, wsrc + 4096, 3072, ""); SB;
        asm volatile("s_waitcnt lgkmcnt(12)" ::: "memory"); SB;
        MF(cacc[6], A87, "v", B2_0_7, "v"); SB;
        MF(cacc[7], A87, "v", B2_1_7, "v"); SB;
        asm volatile("s_waitcnt lgkmcnt(11)" ::: "memory"); SB;
        MF(cacc[7], A88, "v", B2_0_0, "a"); SB;
        GLD4(W6, "v", vo16, wsrc + 8192, 1024, ""); SB;
        asm volatile("s_waitcnt lgkmcnt(10)" ::: "memory"); SB;
        MF(cacc[7], A89, "v", B2_0_1, "a"); SB;
        asm volatile("s_waitcnt lgkmcnt(0)" : "+v"(es0_6), "+v"(es1_6), "+v"(ex_6) :: "memory"); SB;
        float ea_6, eb_6; { float x0 = bflo(ex_6[0]); ea_6 = fmaf(fmaf(x0, es0_6[0], es0_6[1]), cacc[6][0], x0); } PIN(ea_6); SB;
        MF(cacc[7], A90, "v", B2_0_2, "a"); SB;
        { float x1 = bfhi(ex_6[0]); eo_6[0] = pk2(ea_6, fmaf(fmaf(x1, es0_6[2], es0_6[3]), cacc[6][1], x1)); } PIN(eo_6); SB;
        GLD4(W7, "v", vo16, wsrc + 8192, 2048, ""); SB;
        MF(cacc[7], A91, "v", B2_0_3, "a"); SB;
        { float x2 = bflo(ex_6[1]); eb_6 = fmaf(fmaf(x2, es1_6[0], es1_6[1]), cacc[6][2], x2); } PIN(eb_6); SB;
        MF(cacc[7], A92, "v", B2_0_4, "a"); SB;
        { float x3 = bfhi(ex_6[1]); eo_6[1] = pk2(eb_6, fmaf(fmaf(x3, es1_6[2], es1_6[3]), cacc[6][3], x3)); } SB;
        LWR2(pO, eo_6, 4096); SB;
        GLD4(W8, "a", vo16, wsrc + 12288, 0, ""); SB;
        MF(cacc[7], A93, "v", B2_0_5, "a"); SB;
        MF(cacc[7], A94, "v", B2_0_6, "a"); SB;
        f32x4 es0_7, es1_7; u32x2 ex_7, eo_7;
        LRD4(es0_7, "v", pS, 63488); SB;
        LRD4(es1_7, "v", pS, 63504); SB;
        LRD2(ex_7, pX, 3584); SB;
        GLD4(W9, "a", vo16, wsrc + 12288, 1024, ""); SB;
        MF(cacc[7], A95, "v", B2_0_7, "v"); SB;
        STAMP(5);
        asm volatile("s_waitcnt lgkmcnt(0)" : "+v"(es0_7), "+v"(es1_7), "+v"(ex_7) :: "memory"); SB;
        MFNOP(cacc[7]); SB;
        float ea_7, eb_7; { float x0 = bflo(ex_7[0]); ea_7 = fmaf(fmaf(x0, es0_7[0], es0_7[1]), cacc[7][0], x0); } PIN(ea_7); SB;
        { float x1 = bfhi(ex_7[0]); eo_7[0] = pk2(ea_7, fmaf(fmaf(x1, es0_7[2], es0_7[3]), cacc[7][1], x1)); } PIN(eo_7); SB;
        { float x2 = bflo(ex_7[1]); eb_7 = fmaf(fmaf(x2, es1_7[0], es1_7[1]), cacc[7][2], x2); } PIN(eb_7); SB;
        { float x3 = bfhi(ex_7[1]); eo_7[1] = pk2(eb_7, fmaf(fmaf(x3, es1_7[2], es1_7[3]), cacc[7][3], x3)); } SB;
        LWR2(pO, eo_7, 6144); SB;
        asm volatile("s_waitcnt lgkmcnt(0)" ::: "memory"); SB;
        BAR(); SB;

        } else {
        GLD4(W0, "v", vo16, wsrc + 0, 0, "");
        GLD4(W1, "v", vo16, wsrc + 0, 1024, "");
        GLD4(W2, "v", vo16, wsrc + 0, 3072, "");
        GLD4(W3, "v", vo16, wsrc + 4096, 0, "");
        GLD4(W4, "v", vo16, wsrc + 4096, 2048, "");
        GLD4(W5, "v", vo16, wsrc + 4096, 3072, "");
        GLD4(W6, "v", vo16, wsrc + 8192, 1024, "");
        GLD4(W7, "v", vo16, wsrc + 8192, 2048, "");
        GLD4(W8, "a", vo16, wsrc + 12288, 0, "");
        GLD4(W9, "a", vo16, wsrc + 12288, 1024, "");
        if (pi < 60) { BAR(); BAR(); }
        if (w == 0) spin1(ws + FL_A + pi * 128, 0u, 1);
        BAR(); BAR();

        }
        STAMP(6);
        u32x4 V0, V1, V2, V3, V4, V5, V6, V7, V8, V9, V10, V11, V12, V13, V14, V15, V16, V17, V18, V19, V20, V21, V22, V23, V24, V25, V26, V27, V28, V29;
        u32x4 HA0_0, HA0_1, HA1_0, HA1_1, HA2_0, HA2_1, HA3_0, HA3_1, HA4_0, HA4_1, HA5_0, HA5_1;
        u32x4 HB[12];
        u32x2 h0_0_0, h0_0_1, h0_1_0, h0_1_1, h0_2_0, h0_2_1, h0_3_0, h0_3_1, h1_0_0, h1_0_1, h1_1_0, h1_1_1, h1_2_0, h1_2_1, h1_3_0, h1_3_1, h2_0_0, h2_0_1, h2_1_0, h2_1_1, h2_2_0, h2_2_1, h2_3_0, h2_3_1, h3_0_0, h3_0_1, h3_1_0, h3_1_1, h3_2_0, h3_2_1, h3_3_0, h3_3_1, h4_0_0, h4_0_1, h4_1_0, h4_1_1, h4_2_0, h4_2_1, h4_3_0, h4_3_1, h5_0_0, h5_0_1, h5_1_0, h5_1_1, h5_2_0, h5_2_1, h5_3_0, h5_3_1, h6_0_0, h6_0_1, h6_1_0, h6_1_1, h6_2_0, h6_2_1, h6_3_0, h6_3_1, h7_0_0, h7_0_1, h7_1_0, h7_1_1, h7_2_0, h7_2_1, h7_3_0, h7_3_1, h8_0_0, h8_0_1, h8_1_0, h8_1_1, h8_2_0, h8_2_1, h8_3_0, h8_3_1, h9_0_0, h9_0_1, h9_1_0, h9_1_1, h9_2_0, h9_2_1, h9_3_0, h9_3_1, h10_0_0, h10_0_1, h10_1_0, h10_1_1, h10_2_0, h10_2_1, h10_3_0, h10_3_1, h11_0_0, h11_0_1, h11_1_0, h11_1_1, h11_2_0, h11_2_1, h11_3_0, h11_3_1, h12_0_0, h12_0_1, h12_1_0, h12_1_1, h12_2_0, h12_2_1, h12_3_0, h12_3_1, h13_0_0, h13_0_1, h13_1_0, h13_1_1, h13_2_0, h13_2_1, h13_3_0, h13_3_1, h14_0_0, h14_0_1, h14_1_0, h14_1_1, h14_2_0, h14_2_1, h14_3_0, h14_3_1, h15_0_0, h15_0_1, h15_1_0, h15_1_1, h15_2_0, h15_2_1, h15_3_0, h15_3_1, h16_0_0, h16_0_1, h16_1_0, h16_1_1, h16_2_0, h16_2_1, h16_3_0, h16_3_1, h17_0_0, h17_0_1, h17_1_0, h17_1_1, h17_2_0, h17_2_1, h17_3_0, h17_3_1, h18_0_0, h18_0_1, h18_1_0, h18_1_1, h18_2_0, h18_2_1, h18_3_0, h18_3_1, h19_0_0, h19_0_1, h19_1_0, h19_1_1, h19_2_0, h19_2_1, h19_3_0, h19_3_1, h20_0_0, h20_0_1, h20_1_0, h20_1_1, h20_2_0, h20_2_1, h20_3_0, h20_3_1, h21_0_0, h21_0_1, h21_1_0, h21_1_1, h21_2_0, h21_2_1, h21_3_0, h21_3_1, h22_0_0, h22_0_1, h22_1_0, h22_1_1, h22_2_0, h22_2_1, h22_3_0, h22_3_1, h23_0_0, h23_0_1, h23_1_0, h23_1_1, h23_2_0, h23_2_1, h23_3_0, h23_3_1, h24_0_0, h24_0_1, h24_1_0, h24_1_1, h24_2_0, h24_2_1, h24_3_0, h24_3_1, h25_0_0, h25_0_1, h25_1_0, h25_1_1, h25_2_0, h25_2_1, h25_3_0, h25_3_1, h26_0_0, h26_0_1, h26_1_0, h26_1_1, h26_2_0, h26_2_1, h26_3_0, h26_3_1, h27_0_0, h27_0_1, h27_1_0, h27_1_1, h27_2_0, h27_2_1, h27_3_0, h27_3_1, h28_0_0, h28_0_1, h28_1_0, h28_1_1, h28_2_0, h28_2_1, h28_3_0, h28_3_1, h29_0_0, h29_0_1, h29_1_0, h29_1_1, h29_2_0, h29_2_1, h29_3_0, h29_3_1, h30_0_0, h30_0_1, h30_1_0, h30_1_1, h30_2_0, h30_2_1, h30_3_0, h30_3_1, h31_0_0, h31_0_1, h31_1_0, h31_1_1, h31_2_0, h31_2_1, h31_3_0, h31_3_1, h32_0_0, h32_0_1, h32_1_0, h32_1_1, h32_2_0, h32_2_1, h32_3_0, h32_3_1, h33_0_0, h33_0_1, h33_1_0, h33_1_1, h33_2_0, h33_2_1, h33_3_0, h33_3_1, h34_0_0, h34_0_1, h34_1_0, h34_1_1, h34_2_0, h34_2_1, h34_3_0, h34_3_1, h35_0_0, h35_0_1, h35_1_0, h35_1_1, h35_2_0, h35_2_1, h35_3_0, h35_3_1, h36_0_0, h36_0_1, h36_1_0, h36_1_1, h36_2_0, h36_2_1, h36_3_0, h36_3_1, h37_0_0, h37_0_1, h37_1_0, h37_1_1, h37_2_0, h37_2_1, h37_3_0, h37_3_1, h38_0_0, h38_0_1, h38_1_0, h38_1_1, h38_2_0, h38_2_1, h38_3_0, h38_3_1, h39_0_0, h39_0_1, h39_1_0, h39_1_1, h39_2_0, h39_2_1, h39_3_0, h39_3_1;
        u32 fl2, fd_; f32x4 c0_, c1_, c2_;
        f32x4 acc[2][4][2], ssum[2], ssq[2], oc[2][2][5], csv[2];
        asm volatile("s_waitcnt vmcnt(0)" ::: "memory"); SB;
        BAR(); SB;
        STAMP(7);
        LRT2(h0_0_0, hp0, 0); SB;
        LRT2(h0_0_1, hp0, 512); SB;
        LRT2(h0_1_0, hp1, 0); SB;
        LRT2(h0_1_1, hp1, 512); SB;
        __builtin_amdgcn_s_setprio(3); SB;
        asm volatile("s_waitcnt lgkmcnt(0)" ::: "memory"); SB;
        MF0(acc[0][0][0], W0, "v", cat2(h0_0_0, h0_0_1), "v"); SB;
        LRT2(h1_0_0, hp0, 1024); SB;
        MF0(acc[0][0][1], W1, "v", cat2(h0_0_0, h0_0_1), "v"); SB;
        LRT2(h1_0_1, hp0, 1536); SB;
        GLD4(W10, "a", vo16, wsrc + 12288, 3072, ""); SB;
        MF0(acc[0][1][0], W0, "v", cat2(h0_1_0, h0_1_1), "v"); SB;
        LRT2(h1_1_0, hp1, 1024); SB;
        MF0(acc[0][1][1], W1, "v", cat2(h0_1_0, h0_1_1), "v"); SB;
        LRT2(h1_1_1, hp1, 1536); SB;
        GLD4(W11, "a", vo16, wsrc + 16384, 0, ""); SB;
        BAR(); SB;
        __builtin_amdgcn_s_setprio(3); SB;
        asm volatile("s_waitcnt lgkmcnt(0)" ::: "memory"); SB;
        MF(acc[0][0][0], W2, "v", cat2(h1_0_0, h1_0_1), "v"); SB;
        LRT2(h2_0_0, hp0, 2048); SB;
        MF(acc[0][0][1], W3, "v", cat2(h1_0_0, h1_0_1), "v"); SB;
        LRT2(h2_0_1, hp0, 2560); SB;
        GLD4(W12, "a", vo16, wsrc + 16384, 2048, ""); SB;
        MF(acc[0][1][0], W2, "v", cat2(h1_1_0, h1_1_1), "v"); SB;
        LRT2(h2_1_0, hp1, 2048); SB;
        MF(acc[0][1][1], W3, "v", cat2(h1_1_0, h1_1_1), "v"); SB;
        LRT2(h2_1_1, hp1, 2560); SB;
        GLD4(W13, "a", vo16, wsrc + 16384, 3072, ""); SB;
        __builtin_amdgcn_s_setprio(2); SB;
        asm volatile("s_waitcnt lgkmcnt(0)" ::: "memory"); SB;
        MF(acc[0][0][0], W4, "v", cat2(h2_0_0, h2_0_1), "v"); SB;
        LRT2(h3_0_0, hp0, 3072); SB;
        MF(acc[0][0][1], W5, "v", cat2(h2_0_0, h2_0_1), "v"); SB;
        LRT2(h3_0_1, hp0, 3584); SB;
        GLD4(W14, "a", vo16, wsrc + 20480, 1024, ""); SB;
        MF(acc[0][1][0], W4, "v", cat2(h2_1_0, h2_1_1), "v"); SB;
        LRT2(h3_1_0, hp1, 3072); SB;
        MF(acc[0][1][1], W5, "v", cat2(h2_1_0, h2_1_1), "v"); SB;
        LRT2(h3_1_1, hp1, 3584); SB;
        GLD4(W15, "a", vo16, wsrc + 20480, 2048, ""); SB;
        __builtin_amdgcn_s_setprio(1); SB;
        asm volatile("s_waitcnt lgkmcnt(0)" ::: "memory"); SB;
        MF(acc[0][0][0], W6, "v", cat2(h3_0_0, h3_0_1), "v"); SB;
        LRT2(h4_0_0, hp0, 4096); SB;
        MF(acc[0][0][1], W7, "v", cat2(h3_0_0, h3_0_1), "v"); SB;
        LRT2(h4_0_1, hp0, 4608); SB;
        GLD4(W16, "a", vo16, wsrc + 24576, 0, ""); SB;
        MF(acc[0][1][0], W6, "v", cat2(h3_1_0, h3_1_1), "v"); SB;
        LRT2(h4_1_0, hp1, 4096); SB;
        MF(acc[0][1][1], W7, "v", cat2(h3_1_0, h3_1_1), "v"); SB;
        LRT2(h4_1_1, hp1, 4608); SB;
        GLD4(W17, "a", vo16, wsrc + 24576, 1024, ""); SB;
        __builtin_amdgcn_s_setprio(0); SB;
        asm volatile("s_waitcnt lgkmcnt(0)" ::: "memory"); SB;
        MF(acc[0][0][0], W8, "a", cat2(h4_0_0, h4_0_1), "v"); SB;
        LRT2(h0_2_0, hp2, 0); SB;
        MF(acc[0][0][1], W9, "a", cat2(h4_0_0, h4_0_1), "v"); SB;
        LRT2(h0_2_1, hp2, 512); SB;
        GLD4(W18, "a", vo16, wsrc + 24576, 3072, ""); SB;
        MF(acc[0][1][0], W8, "a", cat2(h4_1_0, h4_1_1), "v"); SB;
        LRT2(h0_3_0, hp3, 0); SB;
        MF(acc[0][1][1], W9, "a", cat2(h4_1_0, h4_1_1), "v"); SB;
        LRT2(h0_3_1, hp3, 512); SB;
        GLD4(W19, "a", vo16, wsrc + 28672, 0, ""); SB;
        __builtin_amdgcn_s_setprio(0); SB;
        asm volatile("s_waitcnt lgkmcnt(0)" ::: "memory"); SB;
        MF0(acc[0][2][0], W0, "v", cat2(h0_2_0, h0_2_1), "v"); SB;
        LRT2(h5_0_0, hp0, 5120); SB;
        MF0(acc[0][2][1], W1, "v", cat2(h0_2_0, h0_2_1), "v"); SB;
        LRT2(h5_0_1, hp0, 5632); SB;
        GLD4(W20, "a", vo16, wsrc + 28672, 2048, ""); SB;
        MF0(acc[0][3][0], W0, "v", cat2(h0_3_0, h0_3_1), "v"); SB;
        LRT2(h5_1_0, hp1, 5120); SB;
        MF0(acc[0][3][1], W1, "v", cat2(h0_3_0, h0_3_1), "v"); SB;
        LRT2(h5_1_1, hp1, 5632); SB;
        GLD4(W21, "a", vo16, wsrc + 28672, 3072, ""); SB;
        BAR(); SB;
        asm volatile("s_waitcnt vmcnt(10)" ::: "memory"); SB;
        __builtin_amdgcn_s_setprio(3); SB;
        asm volatile("s_waitcnt lgkmcnt(0)" ::: "memory"); SB;
        MF(acc[0][0][0], W10, "a", cat2(h5_0_0, h5_0_1), "v"); SB;
        LRT2(h1_2_0, hp2, 1024); SB;
        MF(acc[0][0][1], W11, "a", cat2(h5_0_0, h5_0_1), "v"); SB;
        LRT2(h1_2_1, hp2, 1536); SB;
        GLD4(W22, "a", vo16, wsrc + 32768, 1024, ""); SB;
        MF(acc[0][1][0], W10, "a", cat2(h5_1_0, h5_1_1), "v"); SB;
        LRT2(h1_3_0, hp3, 1024); SB;
        MF(acc[0][1][1], W11, "a", cat2(h5_1_0, h5_1_1), "v"); SB;
        LRT2(h1_3_1, hp3, 1536); SB;
        GLD4(W23, "a", vo16, wsrc + 32768, 2048, ""); SB;
        __builtin_amdgcn_s_setprio(2); SB;
        asm volatile("s_waitcnt lgkmcnt(0)" ::: "memory"); SB;
        MF(acc[0][2][0], W2, "v", cat2(h1_2_0, h1_2_1), "v"); SB;
        LRT2(h6_0_0, hp0, 6144); SB;
        MF(acc[0][2][1], W3, "v", cat2(h1_2_0, h1_2_1), "v"); SB;
        LRT2(h6_0_1, hp0, 6656); SB;
        GLD4(W24, "a", vo16, wsrc + 36864, 0, ""); SB;
        MF(acc[0][3][0], W2, "v", cat2(h1_3_0, h1_3_1), "v"); SB;
        LRT2(h6_1_0, hp1, 6144); SB;
        MF(acc[0][3][1], W3, "v", cat2(h1_3_0, h1_3_1), "v"); SB;
        LRT2(h6_1_1, hp1, 6656); SB;
        GLD4(W25, "a", vo16, wsrc + 36864, 1024, ""); SB;
        asm volatile("s_waitcnt vmcnt(12)" ::: "memory"); SB;
        __builtin_amdgcn_s_setprio(1); SB;
        asm volatile("s_waitcnt lgkmcnt(0)" ::: "memory"); SB;
        MF(acc[0][0][0], W12, "a", cat2(h6_0_0, h6_0_1), "v"); SB;
        LRT2(h2_2_0, hp2, 2048); SB;
        MF(acc[0][0][1], W13, "a", cat2(h6_0_0, h6_0_1), "v"); SB;
        LRT2(h2_2_1, hp2, 2560); SB;
        GLD4(W26, "a", vo16, wsrc + 36864, 3072, ""); SB;
        MF(acc[0][1][0], W12, "a", cat2(h6_1_0, h6_1_1), "v"); SB;
        LRT2(h2_3_0, hp3, 2048); SB;
        MF(acc[0][1][1], W13, "a", cat2(h6_1_0, h6_1_1), "v"); SB;
        LRT2(h2_3_1, hp3, 2560); SB;
        GLD4(W27, "a", vo16, wsrc + 40960, 0, ""); SB;
        __builtin_amdgcn_s_setprio(0); SB;
        asm volatile("s_waitcnt lgkmcnt(0)" ::: "memory"); SB;
        MF(acc[0][2][0], W4, "v", cat2(h2_2_0, h2_2_1), "v"); SB;
        LRT2(h7_0_0, hp0, 7168); SB;
        MF(acc[0][2][1], W5, "v", cat2(h2_2_0, h2_2_1), "v"); SB;
        LRT2(h7_0_1, hp0, 7680); SB;
        GLD4(W28, "a", vo16, wsrc + 40960, 2048, ""); SB;
        MF(acc[0][3][0], W4, "v", cat2(h2_3_0, h2_3_1), "v"); SB;
        LRT2(h7_1_0, hp1, 7168); SB;
        MF(acc[0][3][1], W5, "v", cat2(h2_3_0, h2_3_1), "v"); SB;
        LRT2(h7_1_1, hp1, 7680); SB;
        GLD4(W29, "a", vo16, wsrc + 40960, 3072, ""); SB;
        BAR(); SB;
        asm volatile("s_waitcnt vmcnt(14)" ::: "memory"); SB;
        __builtin_amdgcn_s_setprio(3); SB;
        asm volatile("s_waitcnt lgkmcnt(0)" ::: "memory"); SB;
        MF(acc[0][0][0], W14, "a", cat2(h7_0_0, h7_0_1), "v"); SB;
        LRT2(h3_2_0, hp2, 3072); SB;
        MF(acc[0][0][1], W15, "a", cat2(h7_0_0, h7_0_1), "v"); SB;
        LRT2(h3_2_1, hp2, 3584); SB;
        MF(acc[0][1][0], W14, "a", cat2(h7_1_0, h7_1_1), "v"); SB;
        LRT2(h3_3_0, hp3, 3072); SB;
        GLD4(W30, "a", vo16, wsrc + 45056, 1024, ""); SB;
        MF(acc[0][1][1], W15, "a", cat2(h7_1_0, h7_1_1), "v"); SB;
        LRT2(h3_3_1, hp3, 3584); SB;
        __builtin_amdgcn_s_setprio(2); SB;
        asm volatile("s_waitcnt lgkmcnt(0)" ::: "memory"); SB;
        MF(acc[0][2][0], W6, "v", cat2(h3_2_0, h3_2_1), "v"); SB;
        LRT2(h8_0_0, hp0, 8192); SB;
        MF(acc[0][2][1], W7, "v", cat2(h3_2_0, h3_2_1), "v"); SB;
        LRT2(h8_0_1, hp0, 8704); SB;
        MF(acc[0][3][0], W6, "v", cat2(h3_3_0, h3_3_1), "v"); SB;
        LRT2(h8_1_0, hp1, 8192); SB;
        GLD4(W31, "a", vo16, wsrc + 45056, 2048, ""); SB;
        MF(acc[0][3][1], W7, "v", cat2(h3_3_0, h3_3_1), "v"); SB;
        LRT2(h8_1_1, hp1, 8704); SB;
        asm volatile("s_waitcnt vmcnt(14)" ::: "memory"); SB;
        __builtin_amdgcn_s_setprio(1); SB;
        asm volatile("s_waitcnt lgkmcnt(0)" ::: "memory"); SB;
        MF(acc[0][0][0], W16, "a", cat2(h8_0_0, h8_0_1), "v"); SB;
        LRT2(h4_2_0, hp2, 4096); SB;
        MF(acc[0][0][1], W17, "a", cat2(h8_0_0, h8_0_1), "v"); SB;
        LRT2(h4_2_1, hp2, 4608); SB;
        MF(acc[0][1][0], W16, "a", cat2(h8_1_0, h8_1_1), "v"); SB;
        LRT2(h4_3_0, hp3, 4096); SB;
        GLD4(W32, "a", vo16, wsrc + 49152, 0, ""); SB;
        MF(acc[0][1][1], W17, "a", cat2(h8_1_0, h8_1_1), "v"); SB;
        LRT2(h4_3_1, hp3, 4608); SB;
        __builtin_amdgcn_s_setprio(0); SB;
        asm volatile("s_waitcnt lgkmcnt(0)" ::: "memory"); SB;
        MF(acc[0][2][0], W8, "a", cat2(h4_2_0, h4_2_1), "v"); SB;
        LRT2(h9_0_0, hp0, 9216); SB;
        MF(acc[0][2][1], W9, "a", cat2(h4_2_0, h4_2_1), "v"); SB;
        LRT2(h9_0_1, hp0, 9728); SB;
        MF(acc[0][3][0], W8, "a", cat2(h4_3_0, h4_3_1), "v"); SB;
        LRT2(h9_1_0, hp1, 9216); SB;
        GLD4(W33, "a", vo16, wsrc + 49152, 1024, ""); SB;
        MF(acc[0][3][1], W9, "a", cat2(h4_3_0, h4_3_1), "v"); SB;
        LRT2(h9_1_1, hp1, 9728); SB;
        BAR(); SB;
        asm volatile("s_waitcnt vmcnt(14)" ::: "memory"); SB;
        __builtin_amdgcn_s_setprio(3); SB;
        asm volatile("s_waitcnt lgkmcnt(0)" ::: "memory"); SB;
        MF(acc[0][0][0], W18, "a", cat2(h9_0_0, h9_0_1), "v"); SB;
        LRT2(h5_2_0, hp2, 5120); SB;
        MF(acc[0][0][1], W19, "a", cat2(h9_0_0, h9_0_1), "v"); SB;
        LRT2(h5_2_1, hp2, 5632); SB;
        MF(acc[0][1][0], W18, "a", cat2(h9_1_0, h9_1_1), "v"); SB;
        LRT2(h5_3_0, hp3, 5120); SB;
        GLD4(W34, "a", vo16, wsrc + 49152, 3072, ""); SB;
        MF(acc[0][1][1], W19, "a", cat2(h9_1_0, h9_1_1), "v"); SB;
        LRT2(h5_3_1, hp3, 5632); SB;
        __builtin_amdgcn_s_setprio(2); SB;
        asm volatile("s_waitcnt lgkmcnt(0)" ::: "memory"); SB;
        MF(acc[0][2][0], W10, "a", cat2(h5_2_0, h5_2_1), "v"); SB;
        LRT2(h10_0_0, hp0, 10240); SB;
        MF(acc[0][2][1], W11, "a", cat2(h5_2_0, h5_2_1), "v"); SB;
        LRT2(h10_0_1, hp0, 10752); SB;
        MF(acc[0][3][0], W10, "a", cat2(h5_3_0, h5_3_1), "v"); SB;
        LRT2(h10_1_0, hp1, 10240); SB;
        GLD4(W35, "a", vo16, wsrc + 53248, 0, ""); SB;
        MF(acc[0][3][1], W11, "a", cat2(h5_3_0, h5_3_1), "v"); SB;
        LRT2(h10_1_1, hp1, 10752); SB;
        STAMP(8);
        asm volatile("s_waitcnt vmcnt(14)" ::: "memory"); SB;
        __builtin_amdgcn_s_setprio(1); SB;
        asm volatile("s_waitcnt lgkmcnt(0)" ::: "memory"); SB;
        MF(acc[0][0][0], W20, "a", cat2(h10_0_0, h10_0_1), "v"); SB;
        LRT2(h6_2_0, hp2, 6144); SB;
        MF(acc[0][0][1], W21, "a", cat2(h10_0_0, h10_0_1), "v"); SB;
        LRT2(h6_2_1, hp2, 6656); SB;
        MF(acc[0][1][0], W20, "a", cat2(h10_1_0, h10_1_1), "v"); SB;
        LRT2(h6_3_0, hp3, 6144); SB;
        GLD4(W36, "a", vo16, wsrc + 53248, 2048, ""); SB;
        MF(acc[0][1][1], W21, "a", cat2(h10_1_0, h10_1_1), "v"); SB;
        LRT2(h6_3_1, hp3, 6656); SB;
        __builtin_amdgcn_s_setprio(0); SB;
        asm volatile("s_waitcnt lgkmcnt(0)" ::: "memory"); SB;
        MF(acc[0][2][0], W12, "a", cat2(h6_2_0, h6_2_1), "v"); SB;
        LRT2(h11_0_0, hp0, 11264); SB;
        MF(acc[0][2][1], W13, "a", cat2(h6_2_0, h6_2_1), "v"); SB;
        LRT2(h11_0_1, hp0, 11776); SB;
        MF(acc[0][3][0], W12, "a", cat2(h6_3_0, h6_3_1), "v"); SB;
        LRT2(h11_1_0, hp1, 11264); SB;
        GLD4(W37, "a", vo16, wsrc + 53248, 3072, ""); SB;
        MF(acc[0][3][1], W13, "a", cat2(h6_3_0, h6_3_1), "v"); SB;
        LRT2(h11_1_1, hp1, 11776); SB;
        asm volatile("s_waitcnt vmcnt(14)" ::: "memory"); SB;
        __builtin_amdgcn_s_setprio(0); SB;
        asm volatile("s_waitcnt lgkmcnt(0)" ::: "memory"); SB;
        MF(acc[0][0][0], W22, "a", cat2(h11_0_0, h11_0_1), "v"); SB;
        LRT2(h7_2_0, hp2, 7168); SB;
        MF(acc[0][0][1], W23, "a", cat2(h11_0_0, h11_0_1), "v"); SB;
        LRT2(h7_2_1, hp2, 7680); SB;
        MF(acc[0][1][0], W22, "a", cat2(h11_1_0, h11_1_1), "v"); SB;
        LRT2(h7_3_0, hp3, 7168); SB;
        GLD4(W38, "a", vo16, wsrc + 57344, 1024, ""); SB;
        MF(acc[0][1][1], W23, "a", cat2(h11_1_0, h11_1_1), "v"); SB;
        LRT2(h7_3_1, hp3, 7680); SB;
        __builtin_amdgcn_s_setprio(0); SB;
        asm volatile("s_waitcnt lgkmcnt(0)" ::: "memory"); SB;
        MF(acc[0][2][0], W14, "a", cat2(h7_2_0, h7_2_1), "v"); SB;
        LRT2(h12_0_0, hp0, 12288); SB;
        MF(acc[0][2][1], W15, "a", cat2(h7_2_0, h7_2_1), "v"); SB;
        LRT2(h12_0_1, hp0, 12800); SB;
        MF(acc[0][3][0], W14, "a", cat2(h7_3_0, h7_3_1), "v"); SB;
        LRT2(h12_1_0, hp1, 12288); SB;
        GLD4(W39, "a", vo16, wsrc + 57344, 2048, ""); SB;
        MF(acc[0][3][1], W15, "a", cat2(h7_3_0, h7_3_1), "v"); SB;
        LRT2(h12_1_1, hp1, 12800); SB;
        BAR(); SB;
        asm volatile("s_waitcnt vmcnt(14)" ::: "memory"); SB;
        __builtin_amdgcn_s_setprio(3); SB;
        asm volatile("s_waitcnt lgkmcnt(0)" ::: "memory"); SB;
        MF(acc[0][0][0], W24, "a", cat2(h12_0_0, h12_0_1), "v"); SB;
        LRT2(h8_2_0, hp2, 8192); SB;
        MF(acc[0][0][1], W25, "a", cat2(h12_0_0, h12_0_1), "v"); SB;
        LRT2(h8_2_1, hp2, 8704); SB;
        MF(acc[0][1][0], W24, "a", cat2(h12_1_0, h12_1_1), "v"); SB;
        LRT2(h8_3_0, hp3, 8192); SB;
        MF(acc[0][1][1], W25, "a", cat2(h12_1_0, h12_1_1), "v"); SB;
        LRT2(h8_3_1, hp3, 8704); SB;
        __builtin_amdgcn_s_setprio(2); SB;
        asm volatile("s_waitcnt lgkmcnt(0)" ::: "memory"); SB;
        MF(acc[0][2][0], W16, "a", cat2(h8_2_0, h8_2_1), "v"); SB;
        LRT2(h13_0_0, hp0, 13312); SB;
        MF(acc[0][2][1], W17, "a", cat2(h8_2_0, h8_2_1), "v"); SB;
        LRT2(h13_0_1, hp0, 13824); SB;
        MF(acc[0][3][0], W16, "a", cat2(h8_3_0, h8_3_1), "v"); SB;
        LRT2(h13_1_0, hp1, 13312); SB;
        MF(acc[0][3][1], W17, "a", cat2(h8_3_0, h8_3_1), "v"); SB;
        LRT2(h13_1_1, hp1, 13824); SB;
        BAR(); SB;
        asm volatile("s_waitcnt vmcnt(12)" ::: "memory"); SB;
        __builtin_amdgcn_s_setprio(3); SB;
        asm volatile("s_waitcnt lgkmcnt(0)" ::: "memory"); SB;
        MF(acc[0][0][0], W26, "a", cat2(h13_0_0, h13_0_1), "v"); SB;
        LRT2(h9_2_0, hp2, 9216); SB;
        MF(acc[0][0][1], W27, "a", cat2(h13_0_0, h13_0_1), "v"); SB;
        LRT2(h9_2_1, hp2, 9728); SB;
        MF(acc[0][1][0], W26, "a", cat2(h13_1_0, h13_1_1), "v"); SB;
        LRT2(h9_3_0, hp3, 9216); SB;
        MF(acc[0][1][1], W27, "a", cat2(h13_1_0, h13_1_1), "v"); SB;
        LRT2(h9_3_1, hp3, 9728); SB;
        __builtin_amdgcn_s_setprio(2); SB;
        asm volatile("s_waitcnt lgkmcnt(0)" ::: "memory"); SB;
        MF(acc[0][2][0], W18, "a", cat2(h9_2_0, h9_2_1), "v"); SB;
        LRT2(h14_0_0, hp0, 14336); SB;
        MF(acc[0][2][1], W19, "a", cat2(h9_2_0, h9_2_1), "v"); SB;
        LRT2(h14_0_1, hp0, 14848); SB;
        MF(acc[0][3][0], W18, "a", cat2(h9_3_0, h9_3_1), "v"); SB;
        LRT2(h14_1_0, hp1, 14336); SB;
        MF(acc[0][3][1], W19, "a", cat2(h9_3_0, h9_3_1), "v"); SB;
        LRT2(h14_1_1, hp1, 14848); SB;
        asm volatile("s_waitcnt vmcnt(10)" ::: "memory"); SB;
        __builtin_amdgcn_s_setprio(1); SB;
        asm volatile("s_waitcnt lgkmcnt(0)" ::: "memory"); SB;
        MF(acc[0][0][0], W28, "a", cat2(h14_0_0, h14_0_1), "v"); SB;
        LRT2(h10_2_0, hp2, 10240); SB;
        MF(acc[0][0][1], W29, "a", cat2(h14_0_0, h14_0_1), "v"); SB;
        LRT2(h10_2_1, hp2, 10752); SB;
        MF(acc[0][1][0], W28, "a", cat2(h14_1_0, h14_1_1), "v"); SB;
        LRT2(h10_3_0, hp3, 10240); SB;
        MF(acc[0][1][1], W29, "a", cat2(h14_1_0, h14_1_1), "v"); SB;
        LRT2(h10_3_1, hp3, 10752); SB;
        __builtin_amdgcn_s_setprio(0); SB;
        asm volatile("s_waitcnt lgkmcnt(0)" ::: "memory"); SB;
        MF(acc[0][2][0], W20, "a", cat2(h10_2_0, h10_2_1), "v"); SB;
        LRT2(h15_0_0, hp0, 15360); SB;
        MF(acc[0][2][1], W21, "a", cat2(h10_2_0, h10_2_1), "v"); SB;
        LRT2(h15_0_1, hp0, 15872); SB;
        MF(acc[0][3][0], W20, "a", cat2(h10_3_0, h10_3_1), "v"); SB;
        LRT2(h15_1_0, hp1, 15360); SB;
        MF(acc[0][3][1], W21, "a", cat2(h10_3_0, h10_3_1), "v"); SB;
        LRT2(h15_1_1, hp1, 15872); SB;
        asm volatile("s_waitcnt vmcnt(8)" ::: "memory"); SB;
        __builtin_amdgcn_s_setprio(0); SB;
        asm volatile("s_waitcnt lgkmcnt(0)" ::: "memory"); SB;
        MF(acc[0][0][0], W30, "a", cat2(h15_0_0, h15_0_1), "v"); SB;
        LRT2(h11_2_0, hp2, 11264); SB;
        MF(acc[0][0][1], W31, "a", cat2(h15_0_0, h15_0_1), "v"); SB;
        LRT2(h11_2_1, hp2, 11776); SB;
        MF(acc[0][1][0], W30, "a", cat2(h15_1_0, h15_1_1), "v"); SB;
        LRT2(h11_3_0, hp3, 11264); SB;
        MF(acc[0][1][1], W31, "a", cat2(h15_1_0, h15_1_1), "v"); SB;
        LRT2(h11_3_1, hp3, 11776); SB;
        __builtin_amdgcn_s_setprio(0); SB;
        asm volatile("s_waitcnt lgkmcnt(0)" ::: "memory"); SB;
        MF(acc[0][2][0], W22, "a", cat2(h11_2_0, h11_2_1), "v"); SB;
        LRT2(h16_0_0, hp0, 16384); SB;
        MF(acc[0][2][1], W23, "a", cat2(h11_2_0, h11_2_1), "v"); SB;
        LRT2(h16_0_1, hp0, 16896); SB;
        MF(acc[0][3][0], W22, "a", cat2(h11_3_0, h11_3_1), "v"); SB;
        LRT2(h16_1_0, hp1, 16384); SB;
        MF(acc[0][3][1], W23, "a", cat2(h11_3_0, h11_3_1), "v"); SB;
        LRT2(h16_1_1, hp1, 16896); SB;
        asm volatile("s_waitcnt vmcnt(6)" ::: "memory"); SB;
        __builtin_amdgcn_s_setprio(0); SB;
        asm volatile("s_waitcnt lgkmcnt(0)" ::: "memory"); SB;
        MF(acc[0][0][0], W32, "a", cat2(h16_0_0, h16_0_1), "v"); SB;
        LRT2(h12_2_0, hp2, 12288); SB;
        MF(acc[0][0][1], W33, "a", cat2(h16_0_0, h16_0_1), "v"); SB;
        LRT2(h12_2_1, hp2, 12800); SB;
        MF(acc[0][1][0], W32, "a", cat2(h16_1_0, h16_1_1), "v"); SB;
        LRT2(h12_3_0, hp3, 12288); SB;
        MF(acc[0][1][1], W33, "a", cat2(h16_1_0, h16_1_1), "v"); SB;
        LRT2(h12_3_1, hp3, 12800); SB;
        __builtin_amdgcn_s_setprio(0); SB;
        asm volatile("s_waitcnt lgkmcnt(0)" ::: "memory"); SB;
        MF(acc[0][2][0], W24, "a", cat2(h12_2_0, h12_2_1), "v"); SB;
        LRT2(h17_0_0, hp0, 17408); SB;
        MF(acc[0][2][1], W25, "a", cat2(h12_2_0, h12_2_1), "v"); SB;
        LRT2(h17_0_1, hp0, 17920); SB;
        MF(acc[0][3][0], W24, "a", cat2(h12_3_0, h12_3_1), "v"); SB;
        LRT2(h17_1_0, hp1, 17408); SB;
        MF(acc[0][3][1], W25, "a", cat2(h12_3_0, h12_3_1), "v"); SB;
        LRT2(h17_1_1, hp1, 17920); SB;
        BAR(); SB;
        asm volatile("s_waitcnt vmcnt(4)" ::: "memory"); SB;
        __builtin_amdgcn_s_setprio(3); SB;
        asm volatile("s_waitcnt lgkmcnt(0)" ::: "memory"); SB;
        MF(acc[0][0][0], W34, "a", cat2(h17_0_0, h17_0_1), "v"); SB;
        LRT2(h13_2_0, hp2, 13312); SB;
        MF(acc[0][0][1], W35, "a", cat2(h17_0_0, h17_0_1), "v"); SB;
        LRT2(h13_2_1, hp2, 13824); SB;
        MF(acc[0][1][0], W34, "a", cat2(h17_1_0, h17_1_1), "v"); SB;
        LRT2(h13_3_0, hp3, 13312); SB;
        MF(acc[0][1][1], W35, "a", cat2(h17_1_0, h17_1_1), "v"); SB;
        LRT2(h13_3_1, hp3, 13824); SB;
        __builtin_amdgcn_s_setprio(2); SB;
        asm volatile("s_waitcnt lgkmcnt(0)" ::: "memory"); SB;
        MF(acc[0][2][0], W26, "a", cat2(h13_2_0, h13_2_1), "v"); SB;
        LRT2(h18_0_0, hp0, 18432); SB;
        MF(acc[0][2][1], W27, "a", cat2(h13_2_0, h13_2_1), "v"); SB;
        LRT2(h18_0_1, hp0, 18944); SB;
        MF(acc[0][3][0], W26, "a", cat2(h13_3_0, h13_3_1), "v"); SB;
        LRT2(h18_1_0, hp1, 18432); SB;
        MF(acc[0][3][1], W27, "a", cat2(h13_3_0, h13_3_1), "v"); SB;
        LRT2(h18_1_1, hp1, 18944); SB;
        asm volatile("s_waitcnt vmcnt(2)" ::: "memory"); SB;
        __builtin_amdgcn_s_setprio(1); SB;
        asm volatile("s_waitcnt lgkmcnt(0)" ::: "memory"); SB;
        MF(acc[0][0][0], W36, "a", cat2(h18_0_0, h18_0_1), "v"); SB;
        LRT2(h14_2_0, hp2, 14336); SB;
        MF(acc[0][0][1], W37, "a", cat2(h18_0_0, h18_0_1), "v"); SB;
        LRT2(h14_2_1, hp2, 14848); SB;
        MF(acc[0][1][0], W36, "a", cat2(h18_1_0, h18_1_1), "v"); SB;
        LRT2(h14_3_0, hp3, 14336); SB;
        MF(acc[0][1][1], W37, "a", cat2(h18_1_0, h18_1_1), "v"); SB;
        LRT2(h14_3_1, hp3, 14848); SB;
        __builtin_amdgcn_s_setprio(0); SB;
        asm volatile("s_waitcnt lgkmcnt(0)" ::: "memory"); SB;
        MF(acc[0][2][0], W28, "a", cat2(h14_2_0, h14_2_1), "v"); SB;
        LRT2(h19_0_0, hp0, 19456); SB;
        MF(acc[0][2][1], W29, "a", cat2(h14_2_0, h14_2_1), "v"); SB;
        LRT2(h19_0_1, hp0, 19968); SB;
        MF(acc[0][3][0], W28, "a", cat2(h14_3_0, h14_3_1), "v"); SB;
        LRT2(h19_1_0, hp1, 19456); SB;
        MF(acc[0][3][1], W29, "a", cat2(h14_3_0, h14_3_1), "v"); SB;
        LRT2(h19_1_1, hp1, 19968); SB;
        asm volatile("s_waitcnt vmcnt(0)" ::: "memory"); SB;
        __builtin_amdgcn_s_setprio(0); SB;
        asm volatile("s_waitcnt lgkmcnt(0)" ::: "memory"); SB;
        MF(acc[0][0][0], W38, "a", cat2(h19_0_0, h19_0_1), "v"); SB;
        LRT2(h15_2_0, hp2, 15360); SB;
        MF(acc[0][0][1], W39, "a", cat2(h19_0_0, h19_0_1), "v"); SB;
        LRT2(h15_2_1, hp2, 15872); SB;
        MF(acc[0][1][0], W38, "a", cat2(h19_1_0, h19_1_1), "v"); SB;
        LRT2(h15_3_0, hp3, 15360); SB;
        MF(acc[0][1][1], W39, "a", cat2(h19_1_0, h19_1_1), "v"); SB;
        LRT2(h15_3_1, hp3, 15872); SB;
        __builtin_amdgcn_s_setprio(0); SB;
        asm volatile("s_waitcnt lgkmcnt(0)" ::: "memory"); SB;
        MF(acc[0][2][0], W30, "a", cat2(h15_2_0, h15_2_1), "v"); SB;
        LRT2(h16_2_0, hp2, 16384); SB;
        MF(acc[0][2][1], W31, "a", cat2(h15_2_0, h15_2_1), "v"); SB;
        LRT2(h16_2_1, hp2, 16896); SB;
        MF(acc[0][3][0], W30, "a", cat2(h15_3_0, h15_3_1), "v"); SB;
        LRT2(h16_3_0, hp3, 16384); SB;
        MF(acc[0][3][1], W31, "a", cat2(h15_3_0, h15_3_1), "v"); SB;
        LRT2(h16_3_1, hp3, 16896); SB;
        STAMP(9);
        __builtin_amdgcn_s_setprio(0); SB;
        asm volatile("s_waitcnt lgkmcnt(0)" ::: "memory"); SB;
        MF(acc[0][2][0], W32, "a", cat2(h16_2_0, h16_2_1), "v"); SB;
        LRT2(h17_2_0, hp2, 17408); SB;
        MF(acc[0][2][1], W33, "a", cat2(h16_2_0, h16_2_1), "v"); SB;
        LRT2(h17_2_1, hp2, 17920); SB;
        MF(acc[0][3][0], W32, "a", cat2(h16_3_0, h16_3_1), "v"); SB;
        LRT2(h17_3_0, hp3, 17408); SB;
        MF(acc[0][3][1], W33, "a", cat2(h16_3_0, h16_3_1), "v"); SB;
        LRT2(h17_3_1, hp3, 17920); SB;
        BAR(); SB;
        __builtin_amdgcn_s_setprio(3); SB;
        asm volatile("s_waitcnt lgkmcnt(0)" ::: "memory"); SB;
        MF(acc[0][2][0], W34, "a", cat2(h17_2_0, h17_2_1), "v"); SB;
        LRT2(h18_2_0, hp2, 18432); SB;
        f32x2 st0_0, st0_1, st0_2, st0_3;
        LRD2(st0_0, sp0, 0); SB;
        LRD2(st0_1, sp1, 0); SB;
        LRD2(st0_2, sp2, 0); SB;
        LRD2(st0_3, sp3, 0); SB;
        LRD4(csv[0], "v", cp, 0); SB;
        LRD4(csv[1], "v", cp, 64); SB;
        MF(acc[0][2][1], W35, "a", cat2(h17_2_0, h17_2_1), "v"); SB;
        LRT2(h18_2_1, hp2, 18944); SB;
        MF(acc[0][3][0], W34, "a", cat2(h17_3_0, h17_3_1), "v"); SB;
        LRT2(h18_3_0, hp3, 18432); SB;
        asm volatile("s_waitcnt lgkmcnt(3)" ::: "memory"); SB;
        float z0_0_0a = acc[0][0][0][0], z0_0_0b = acc[0][0][0][1], z0_0_0c = acc[0][0][0][2], z0_0_0d = acc[0][0][0][3];
        EPI_A(z0_0_0a, z0_0_0c, st0_0[0], csv[0][0], csv[0][2]); EPI_A(z0_0_0b, z0_0_0d, st0_0[0], csv[0][1], csv[0][3]); SB;
        MF(acc[0][3][1], W35, "a", cat2(h17_3_0, h17_3_1), "v"); SB;
        LRT2(h18_3_1, hp3, 18944); SB;
        EPI_B(z0_0_0a, z0_0_0c, st0_0[1]); EPI_B(z0_0_0b, z0_0_0d, st0_0[1]); SB;
        __builtin_amdgcn_s_setprio(2); SB;
        asm volatile("s_waitcnt lgkmcnt(0)" ::: "memory"); SB;
        MF(acc[0][2][0], W36, "a", cat2(h18_2_0, h18_2_1), "v"); SB;
        LRT2(h19_2_0, hp2, 19456); SB;
        EPI_C(z0_0_0a, z0_0_0b, z0_0_0c, z0_0_0d); SB;
        EPI_D(z0_0_0a, z0_0_0b, xp0, 0); SB;
        MF(acc[0][2][1], W37, "a", cat2(h18_2_0, h18_2_1), "v"); SB;
        LRT2(h19_2_1, hp2, 19968); SB;
        float z0_0_1a = acc[0][0][1][0], z0_0_1b = acc[0][0][1][1], z0_0_1c = acc[0][0][1][2], z0_0_1d = acc[0][0][1][3];
        EPI_A(z0_0_1a, z0_0_1c, st0_0[0], csv[1][0], csv[1][2]); EPI_A(z0_0_1b, z0_0_1d, st0_0[0], csv[1][1], csv[1][3]); SB;
        MF(acc[0][3][0], W36, "a", cat2(h18_3_0, h18_3_1), "v"); SB;
        LRT2(h19_3_0, hp3, 19456); SB;
        EPI_B(z0_0_1a, z0_0_1c, st0_0[1]); EPI_B(z0_0_1b, z0_0_1d, st0_0[1]); SB;
        MF(acc[0][3][1], W37, "a", cat2(h18_3_0, h18_3_1), "v"); SB;
        LRT2(h19_3_1, hp3, 19968); SB;
        EPI_C(z0_0_1a, z0_0_1b, z0_0_1c, z0_0_1d); SB;
        EPI_D(z0_0_1a, z0_0_1b, xp0, 256); SB;
        BAR(); SB;
        __builtin_amdgcn_s_setprio(3); SB;
        asm volatile("s_waitcnt lgkmcnt(1)" ::: "memory"); SB;
        MF(acc[0][2][0], W38, "a", cat2(h19_2_0, h19_2_1), "v"); SB;
        LRT2(h20_0_0, hp0, 0); SB;
        float z0_1_0a = acc[0][1][0][0], z0_1_0b = acc[0][1][0][1], z0_1_0c = acc[0][1][0][2], z0_1_0d = acc[0][1][0][3];
        EPI_A(z0_1_0a, z0_1_0c, st0_1[0], csv[0][0], csv[0][2]); EPI_A(z0_1_0b, z0_1_0d, st0_1[0], csv[0][1], csv[0][3]); SB;
        MF(acc[0][2][1], W39, "a", cat2(h19_2_0, h19_2_1), "v"); SB;
        LRT2(h20_0_1, hp0, 512); SB;
        EPI_B(z0_1_0a, z0_1_0c, st0_1[1]); EPI_B(z0_1_0b, z0_1_0d, st0_1[1]); SB;
        MF(acc[0][3][0], W38, "a", cat2(h19_3_0, h19_3_1), "v"); SB;
        LRT2(h20_1_0, hp1, 0); SB;
        EPI_C(z0_1_0a, z0_1_0b, z0_1_0c, z0_1_0d); SB;
        EPI_D(z0_1_0a, z0_1_0b, xp1, 0); SB;
        MF(acc[0][3][1], W39, "a", cat2(h19_3_0, h19_3_1), "v"); SB;
        LRT2(h20_1_1, hp1, 512); SB;
        float z0_1_1a = acc[0][1][1][0], z0_1_1b = acc[0][1][1][1], z0_1_1c = acc[0][1][1][2], z0_1_1d = acc[0][1][1][3];
        EPI_A(z0_1_1a, z0_1_1c, st0_1[0], csv[1][0], csv[1][2]); EPI_A(z0_1_1b, z0_1_1d, st0_1[0], csv[1][1], csv[1][3]); SB;
        EPI_B(z0_1_1a, z0_1_1c, st0_1[1]); EPI_B(z0_1_1b, z0_1_1d, st0_1[1]); SB;
        EPI_C(z0_1_1a, z0_1_1b, z0_1_1c, z0_1_1d); SB;
        EPI_D(z0_1_1a, z0_1_1b, xp1, 256); SB;
        __builtin_amdgcn_s_setprio(2); SB;
        asm volatile("s_waitcnt lgkmcnt(1)" ::: "memory"); SB;
        MF0(acc[1][0][0], W0, "v", cat2(h20_0_0, h20_0_1), "v"); SB;
        LRT2(h21_0_0, hp0, 1024); SB;
        MF0(acc[1][0][1], W1, "v", cat2(h20_0_0, h20_0_1), "v"); SB;
        LRT2(h21_0_1, hp0, 1536); SB;
        float z0_2_0a = acc[0][2][0][0], z0_2_0b = acc[0][2][0][1], z0_2_0c = acc[0][2][0][2], z0_2_0d = acc[0][2][0][3];
        EPI_A(z0_2_0a, z0_2_0c, st0_2[0], csv[0][0], csv[0][2]); EPI_A(z0_2_0b, z0_2_0d, st0_2[0], csv[0][1], csv[0][3]); SB;
        MF0(acc[1][1][0], W0, "v", cat2(h20_1_0, h20_1_1), "v"); SB;
        LRT2(h21_1_0, hp1, 1024); SB;
        EPI_B(z0_2_0a, z0_2_0c, st0_2[1]); EPI_B(z0_2_0b, z0_2_0d, st0_2[1]); SB;
        MF0(acc[1][1][1], W1, "v", cat2(h20_1_0, h20_1_1), "v"); SB;
        LRT2(h21_1_1, hp1, 1536); SB;
        EPI_C(z0_2_0a, z0_2_0b, z0_2_0c, z0_2_0d); SB;
        EPI_D(z0_2_0a, z0_2_0b, xp2, 0); SB;
        __builtin_amdgcn_s_setprio(1); SB;
        asm volatile("s_waitcnt lgkmcnt(1)" ::: "memory"); SB;
        MF(acc[1][0][0], W2, "v", cat2(h21_0_0, h21_0_1), "v"); SB;
        LRT2(h22_0_0, hp0, 2048); SB;
        float z0_2_1a = acc[0][2][1][0], z0_2_1b = acc[0][2][1][1], z0_2_1c = acc[0][2][1][2], z0_2_1d = acc[0][2][1][3];
        EPI_A(z0_2_1a, z0_2_1c, st0_2[0], csv[1][0], csv[1][2]); EPI_A(z0_2_1b, z0_2_1d, st0_2[0], csv[1][1], csv[1][3]); SB;
        MF(acc[1][0][1], W3, "v", cat2(h21_0_0, h21_0_1), "v"); SB;
        LRT2(h22_0_1, hp0, 2560); SB;
        EPI_B(z0_2_1a, z0_2_1c, st0_2[1]); EPI_B(z0_2_1b, z0_2_1d, st0_2[1]); SB;
        MF(acc[1][1][0], W2, "v", cat2(h21_1_0, h21_1_1), "v"); SB;
        LRT2(h22_1_0, hp1, 2048); SB;
        EPI_C(z0_2_1a, z0_2_1b, z0_2_1c, z0_2_1d); SB;
        EPI_D(z0_2_1a, z0_2_1b, xp2, 256); SB;
        MF(acc[1][1][1], W3, "v", cat2(h21_1_0, h21_1_1), "v"); SB;
        LRT2(h22_1_1, hp1, 2560); SB;
        float z0_3_0a = acc[0][3][0][0], z0_3_0b = acc[0][3][0][1], z0_3_0c = acc[0][3][0][2], z0_3_0d = acc[0][3][0][3];
        EPI_A(z0_3_0a, z0_3_0c, st0_3[0], csv[0][0], csv[0][2]); EPI_A(z0_3_0b, z0_3_0d, st0_3[0], csv[0][1], csv[0][3]); SB;
        __builtin_amdgcn_s_setprio(0); SB;
        asm volatile("s_waitcnt lgkmcnt(0)" ::: "memory"); SB;
        MF(acc[1][0][0], W4, "v", cat2(h22_0_0, h22_0_1), "v"); SB;
        LRT2(h23_0_0, hp0, 3072); SB;
        EPI_B(z0_3_0a, z0_3_0c, st0_3[1]); EPI_B(z0_3_0b, z0_3_0d, st0_3[1]); SB;
        MF(acc[1][0][1], W5, "v", cat2(h22_0_0, h22_0_1), "v"); SB;
        LRT2(h23_0_1, hp0, 3584); SB;
        EPI_C(z0_3_0a, z0_3_0b, z0_3_0c, z0_3_0d); SB;
        EPI_D(z0_3_0a, z0_3_0b, xp3, 0); SB;
        MF(acc[1][1][0], W4, "v", cat2(h22_1_0, h22_1_1), "v"); SB;
        LRT2(h23_1_0, hp1, 3072); SB;
        float z0_3_1a = acc[0][3][1][0], z0_3_1b = acc[0][3][1][1], z0_3_1c = acc[0][3][1][2], z0_3_1d = acc[0][3][1][3];
        EPI_A(z0_3_1a, z0_3_1c, st0_3[0], csv[1][0], csv[1][2]); EPI_A(z0_3_1b, z0_3_1d, st0_3[0], csv[1][1], csv[1][3]); SB;
        MF(acc[1][1][1], W5, "v", cat2(h22_1_0, h22_1_1), "v"); SB;
        LRT2(h23_1_1, hp1, 3584); SB;
        EPI_B(z0_3_1a, z0_3_1c, st0_3[1]); EPI_B(z0_3_1b, z0_3_1d, st0_3[1]); SB;
        BAR(); SB;
        __builtin_amdgcn_s_setprio(3); SB;
        asm volatile("s_waitcnt lgkmcnt(0)" ::: "memory"); SB;
        MF(acc[1][0][0], W6, "v", cat2(h23_0_0, h23_0_1), "v"); SB;
        LRT2(h24_0_0, hp0, 4096); SB;
        EPI_C(z0_3_1a, z0_3_1b, z0_3_1c, z0_3_1d); SB;
        EPI_D(z0_3_1a, z0_3_1b, xp3, 256); SB;
        MF(acc[1][0][1], W7, "v", cat2(h23_0_0, h23_0_1), "v"); SB;
        LRT2(h24_0_1, hp0, 4608); SB;
        MF(acc[1][1][0], W6, "v", cat2(h23_1_0, h23_1_1), "v"); SB;
        LRT2(h24_1_0, hp1, 4096); SB;
        MF(acc[1][1][1], W7, "v", cat2(h23_1_0, h23_1_1), "v"); SB;
        LRT2(h24_1_1, hp1, 4608); SB;
        STAMP(11);
        asm volatile("s_waitcnt lgkmcnt(3)" ::: "memory"); SB;
        BAR(); SB;
        __builtin_amdgcn_s_setprio(3); SB;
        asm volatile("s_waitcnt lgkmcnt(0)" ::: "memory"); SB;
        MF(acc[1][0][0], W8, "a", cat2(h24_0_0, h24_0_1), "v"); SB;
        LRT2(h20_2_0, hp2, 0); SB;
        MF(acc[1][0][1], W9, "a", cat2(h24_0_0, h24_0_1), "v"); SB;
        LRT2(h20_2_1, hp2, 512); SB;
        MF(acc[1][1][0], W8, "a", cat2(h24_1_0, h24_1_1), "v"); SB;
        LRT2(h20_3_0, hp3, 0); SB;
        MF(acc[1][1][1], W9, "a", cat2(h24_1_0, h24_1_1), "v"); SB;
        LRT2(h20_3_1, hp3, 512); SB;
        __builtin_amdgcn_s_setprio(2); SB;
        asm volatile("s_waitcnt lgkmcnt(0)" ::: "memory"); SB;
        MF0(acc[1][2][0], W0, "v", cat2(h20_2_0, h20_2_1), "v"); SB;
        LRT2(h25_0_0, hp0, 5120); SB;
        MF0(acc[1][2][1], W1, "v", cat2(h20_2_0, h20_2_1), "v"); SB;
        LRT2(h25_0_1, hp0, 5632); SB;
        MF0(acc[1][3][0], W0, "v", cat2(h20_3_0, h20_3_1), "v"); SB;
        LRT2(h25_1_0, hp1, 5120); SB;
        MF0(acc[1][3][1], W1, "v", cat2(h20_3_0, h20_3_1), "v"); SB;
        LRT2(h25_1_1, hp1, 5632); SB;
        __builtin_amdgcn_s_setprio(1); SB;
        asm volatile("s_waitcnt lgkmcnt(0)" ::: "memory"); SB;
        MF(acc[1][0][0], W10, "a", cat2(h25_0_0, h25_0_1), "v"); SB;
        LRT2(h21_2_0, hp2, 1024); SB;
        MF(acc[1][0][1], W11, "a", cat2(h25_0_0, h25_0_1), "v"); SB;
        LRT2(h21_2_1, hp2, 1536); SB;
        MF(acc[1][1][0], W10, "a", cat2(h25_1_0, h25_1_1), "v"); SB;
        LRT2(h21_3_0, hp3, 1024); SB;
        MF(acc[1][1][1], W11, "a", cat2(h25_1_0, h25_1_1), "v"); SB;
        LRT2(h21_3_1, hp3, 1536); SB;
        __builtin_amdgcn_s_setprio(0); SB;
        asm volatile("s_waitcnt lgkmcnt(0)" ::: "memory"); SB;
        MF(acc[1][2][0], W2, "v", cat2(h21_2_0, h21_2_1), "v"); SB;
        LRT2(h26_0_0, hp0, 6144); SB;
        MF(acc[1][2][1], W3, "v", cat2(h21_2_0, h21_2_1), "v"); SB;
        LRT2(h26_0_1, hp0, 6656); SB;
        MF(acc[1][3][0], W2, "v", cat2(h21_3_0, h21_3_1), "v"); SB;
        LRT2(h26_1_0, hp1, 6144); SB;
        MF(acc[1][3][1], W3, "v", cat2(h21_3_0, h21_3_1), "v"); SB;
        LRT2(h26_1_1, hp1, 6656); SB;
        __builtin_amdgcn_s_setprio(0); SB;
        asm volatile("s_waitcnt lgkmcnt(0)" ::: "memory"); SB;
        MF(acc[1][0][0], W12, "a", cat2(h26_0_0, h26_0_1), "v"); SB;
        LRT2(h22_2_0, hp2, 2048); SB;
        MF(acc[1][0][1], W13, "a", cat2(h26_0_0, h26_0_1), "v"); SB;
        LRT2(h22_2_1, hp2, 2560); SB;
        MF(acc[1][1][0], W12, "a", cat2(h26_1_0, h26_1_1), "v"); SB;
        LRT2(h22_3_0, hp3, 2048); SB;
        MF(acc[1][1][1], W13, "a", cat2(h26_1_0, h26_1_1), "v"); SB;
        LRT2(h22_3_1, hp3, 2560); SB;
        __builtin_amdgcn_s_setprio(0); SB;
        asm volatile("s_waitcnt lgkmcnt(0)" ::: "memory"); SB;
        MF(acc[1][2][0], W4, "v", cat2(h22_2_0, h22_2_1), "v"); SB;
        LRT2(h27_0_0, hp0, 7168); SB;
        MF(acc[1][2][1], W5, "v", cat2(h22_2_0, h22_2_1), "v"); SB;
        LRT2(h27_0_1, hp0, 7680); SB;
        MF(acc[1][3][0], W4, "v", cat2(h22_3_0, h22_3_1), "v"); SB;
        LRT2(h27_1_0, hp1, 7168); SB;
        MF(acc[1][3][1], W5, "v", cat2(h22_3_0, h22_3_1), "v"); SB;
        LRT2(h27_1_1, hp1, 7680); SB;
        BAR(); SB;
        __builtin_amdgcn_s_setprio(3); SB;
        asm volatile("s_waitcnt lgkmcnt(0)" ::: "memory"); SB;
        MF(acc[1][0][0], W14, "a", cat2(h27_0_0, h27_0_1), "v"); SB;
        LRT2(h23_2_0, hp2, 3072); SB;
        MF(acc[1][0][1], W15, "a", cat2(h27_0_0, h27_0_1), "v"); SB;
        LRT2(h23_2_1, hp2, 3584); SB;
        MF(acc[1][1][0], W14, "a", cat2(h27_1_0, h27_1_1), "v"); SB;
        LRT2(h23_3_0, hp3, 3072); SB;
        MF(acc[1][1][1], W15, "a", cat2(h27_1_0, h27_1_1), "v"); SB;
        LRT2(h23_3_1, hp3, 3584); SB;
        __builtin_amdgcn_s_setprio(2); SB;
        asm volatile("s_waitcnt lgkmcnt(0)" ::: "memory"); SB;
        MF(acc[1][2][0], W6, "v", cat2(h23_2_0, h23_2_1), "v"); SB;
        LRT2(h28_0_0, hp0, 8192); SB;
        MF(acc[1][2][1], W7, "v", cat2(h23_2_0, h23_2_1), "v"); SB;
        LRT2(h28_0_1, hp0, 8704); SB;
        MF(acc[1][3][0], W6, "v", cat2(h23_3_0, h23_3_1), "v"); SB;
        LRT2(h28_1_0, hp1, 8192); SB;
        MF(acc[1][3][1], W7, "v", cat2(h23_3_0, h23_3_1), "v"); SB;
        LRT2(h28_1_1, hp1, 8704); SB;
        __builtin_amdgcn_s_setprio(1); SB;
        asm volatile("s_waitcnt lgkmcnt(0)" ::: "memory"); SB;
        MF(acc[1][0][0], W16, "a", cat2(h28_0_0, h28_0_1), "v"); SB;
        LRT2(h24_2_0, hp2, 4096); SB;
        MF(acc[1][0][1], W17, "a", cat2(h28_0_0, h28_0_1), "v"); SB;
        LRT2(h24_2_1, hp2, 4608); SB;
        MF(acc[1][1][0], W16, "a", cat2(h28_1_0, h28_1_1), "v"); SB;
        LRT2(h24_3_0, hp3, 4096); SB;
        MF(acc[1][1][1], W17, "a", cat2(h28_1_0, h28_1_1), "v"); SB;
        LRT2(h24_3_1, hp3, 4608); SB;
        __builtin_amdgcn_s_setprio(0); SB;
        asm volatile("s_waitcnt lgkmcnt(0)" ::: "memory"); SB;
        MF(acc[1][2][0], W8, "a", cat2(h24_2_0, h24_2_1), "v"); SB;
        LRT2(h29_0_0, hp0, 9216); SB;
        MF(acc[1][2][1], W9, "a", cat2(h24_2_0, h24_2_1), "v"); SB;
        LRT2(h29_0_1, hp0, 9728); SB;
        MF(acc[1][3][0], W8, "a", cat2(h24_3_0, h24_3_1), "v"); SB;
        LRT2(h29_1_0, hp1, 9216); SB;
        MF(acc[1][3][1], W9, "a", cat2(h24_3_0, h24_3_1), "v"); SB;
        LRT2(h29_1_1, hp1, 9728); SB;
        __builtin_amdgcn_s_setprio(0); SB;
        asm volatile("s_waitcnt lgkmcnt(0)" ::: "memory"); SB;
        MF(acc[1][0][0], W18, "a", cat2(h29_0_0, h29_0_1), "v"); SB;
        LRT2(h25_2_0, hp2, 5120); SB;
        MF(acc[1][0][1], W19, "a", cat2(h29_0_0, h29_0_1), "v"); SB;
        LRT2(h25_2_1, hp2, 5632); SB;
        MF(acc[1][1][0], W18, "a", cat2(h29_1_0, h29_1_1), "v"); SB;
        LRT2(h25_3_0, hp3, 5120); SB;
        LRD4(V0, "a", vlr, 0); SB;
        MF(acc[1][1][1], W19, "a", cat2(h29_1_0, h29_1_1), "v"); SB;
        LRT2(h25_3_1, hp3, 5632); SB;
        __builtin_amdgcn_s_setprio(0); SB;
        asm volatile("s_waitcnt lgkmcnt(0)" ::: "memory"); SB;
        MF(acc[1][2][0], W10, "a", cat2(h25_2_0, h25_2_1), "v"); SB;
        LRT2(h30_0_0, hp0, 10240); SB;
        MF(acc[1][2][1], W11, "a", cat2(h25_2_0, h25_2_1), "v"); SB;
        LRT2(h30_0_1, hp0, 10752); SB;
        MF(acc[1][3][0], W10, "a", cat2(h25_3_0, h25_3_1), "v"); SB;
        LRT2(h30_1_0, hp1, 10240); SB;
        LRD4(V1, "a", vlr, 1024); SB;
        MF(acc[1][3][1], W11, "a", cat2(h25_3_0, h25_3_1), "v"); SB;
        LRT2(h30_1_1, hp1, 10752); SB;
        __builtin_amdgcn_s_setprio(0); SB;
        asm volatile("s_waitcnt lgkmcnt(0)" ::: "memory"); SB;
        MF(acc[1][0][0], W20, "a", cat2(h30_0_0, h30_0_1), "v"); SB;
        LRT2(h26_2_0, hp2, 6144); SB;
        MF(acc[1][0][1], W21, "a", cat2(h30_0_0, h30_0_1), "v"); SB;
        LRT2(h26_2_1, hp2, 6656); SB;
        MF(acc[1][1][0], W20, "a", cat2(h30_1_0, h30_1_1), "v"); SB;
        LRT2(h26_3_0, hp3, 6144); SB;
        LRD4(V2, "a", vlr, 2048); SB;
        MF(acc[1][1][1], W21, "a", cat2(h30_1_0, h30_1_1), "v"); SB;
        LRT2(h26_3_1, hp3, 6656); SB;
        __builtin_amdgcn_s_setprio(0); SB;
        asm volatile("s_waitcnt lgkmcnt(0)" ::: "memory"); SB;
        MF(acc[1][2][0], W12, "a", cat2(h26_2_0, h26_2_1), "v"); SB;
        LRT2(h31_0_0, hp0, 11264); SB;
        MF(acc[1][2][1], W13, "a", cat2(h26_2_0, h26_2_1), "v"); SB;
        LRT2(h31_0_1, hp0, 11776); SB;
        MF(acc[1][3][0], W12, "a", cat2(h26_3_0, h26_3_1), "v"); SB;
        LRT2(h31_1_0, hp1, 11264); SB;
        LRD4(V3, "a", vlr, 3072); SB;
        MF(acc[1][3][1], W13, "a", cat2(h26_3_0, h26_3_1), "v"); SB;
        LRT2(h31_1_1, hp1, 11776); SB;
        BAR(); SB;
        __builtin_amdgcn_s_setprio(3); SB;
        asm volatile("s_waitcnt lgkmcnt(0)" ::: "memory"); SB;
        MF(acc[1][0][0], W22, "a", cat2(h31_0_0, h31_0_1), "v"); SB;
        LRT2(h27_2_0, hp2, 7168); SB;
        MF(acc[1][0][1], W23, "a", cat2(h31_0_0, h31_0_1), "v"); SB;
        LRT2(h27_2_1, hp2, 7680); SB;
        MF(acc[1][1][0], W22, "a", cat2(h31_1_0, h31_1_1), "v"); SB;
        LRT2(h27_3_0, hp3, 7168); SB;
        LRD4(V4, "a", vlr, 4096); SB;
        MF(acc[1][1][1], W23, "a", cat2(h31_1_0, h31_1_1), "v"); SB;
        LRT2(h27_3_1, hp3, 7680); SB;
        __builtin_amdgcn_s_setprio(2); SB;
        asm volatile("s_waitcnt lgkmcnt(0)" ::: "memory"); SB;
        MF(acc[1][2][0], W14, "a", cat2(h27_2_0, h27_2_1), "v"); SB;
        LRT2(h32_0_0, hp0, 12288); SB;
        MF(acc[1][2][1], W15, "a", cat2(h27_2_0, h27_2_1), "v"); SB;
        LRT2(h32_0_1, hp0, 12800); SB;
        MF(acc[1][3][0], W14, "a", cat2(h27_3_0, h27_3_1), "v"); SB;
        LRT2(h32_1_0, hp1, 12288); SB;
        LRD4(V5, "a", vlr, 5120); SB;
        MF(acc[1][3][1], W15, "a", cat2(h27_3_0, h27_3_1), "v"); SB;
        LRT2(h32_1_1, hp1, 12800); SB;
        __builtin_amdgcn_s_setprio(1); SB;
        asm volatile("s_waitcnt lgkmcnt(0)" ::: "memory"); SB;
        MF(acc[1][0][0], W24, "a", cat2(h32_0_0, h32_0_1), "v"); SB;
        LRT2(h28_2_0, hp2, 8192); SB;
        MF(acc[1][0][1], W25, "a", cat2(h32_0_0, h32_0_1), "v"); SB;
        LRT2(h28_2_1, hp2, 8704); SB;
        MF(acc[1][1][0], W24, "a", cat2(h32_1_0, h32_1_1), "v"); SB;
        LRT2(h28_3_0, hp3, 8192); SB;
        LRD4(V6, "a", vlr, 6144); SB;
        MF(acc[1][1][1], W25, "a", cat2(h32_1_0, h32_1_1), "v"); SB;
        LRT2(h28_3_1, hp3, 8704); SB;
        __builtin_amdgcn_s_setprio(0); SB;
        asm volatile("s_waitcnt lgkmcnt(0)" ::: "memory"); SB;
        MF(acc[1][2][0], W16, "a", cat2(h28_2_0, h28_2_1), "v"); SB;
        LRT2(h33_0_0, hp0, 13312); SB;
        MF(acc[1][2][1], W17, "a", cat2(h28_2_0, h28_2_1), "v"); SB;
        LRT2(h33_0_1, hp0, 13824); SB;
        MF(acc[1][3][0], W16, "a", cat2(h28_3_0, h28_3_1), "v"); SB;
        LRT2(h33_1_0, hp1, 13312); SB;
        LRD4(V7, "a", vlr, 7168); SB;
        MF(acc[1][3][1], W17, "a", cat2(h28_3_0, h28_3_1), "v"); SB;
        LRT2(h33_1_1, hp1, 13824); SB;
        __builtin_amdgcn_s_setprio(0); SB;
        asm volatile("s_waitcnt lgkmcnt(0)" ::: "memory"); SB;
        MF(acc[1][0][0], W26, "a", cat2(h33_0_0, h33_0_1), "v"); SB;
        LRT2(h29_2_0, hp2, 9216); SB;
        MF(acc[1][0][1], W27, "a", cat2(h33_0_0, h33_0_1), "v"); SB;
        LRT2(h29_2_1, hp2, 9728); SB;
        MF(acc[1][1][0], W26, "a", cat2(h33_1_0, h33_1_1), "v"); SB;
        LRT2(h29_3_0, hp3, 9216); SB;
        LRD4(V8, "a", vlr, 8192); SB;
        MF(acc[1][1][1], W27, "a", cat2(h33_1_0, h33_1_1), "v"); SB;
        LRT2(h29_3_1, hp3, 9728); SB;
        __builtin_amdgcn_s_setprio(0); SB;
        asm volatile("s_waitcnt lgkmcnt(0)" ::: "memory"); SB;
        MF(acc[1][2][0], W18, "a", cat2(h29_2_0, h29_2_1), "v"); SB;
        LRT2(h34_0_0, hp0, 14336); SB;
        MF(acc[1][2][1], W19, "a", cat2(h29_2_0, h29_2_1), "v"); SB;
        LRT2(h34_0_1, hp0, 14848); SB;
        MF(acc[1][3][0], W18, "a", cat2(h29_3_0, h29_3_1), "v"); SB;
        LRT2(h34_1_0, hp1, 14336); SB;
        LRD4(V9, "a", vlr, 9216); SB;
        MF(acc[1][3][1], W19, "a", cat2(h29_3_0, h29_3_1), "v"); SB;
        LRT2(h34_1_1, hp1, 14848); SB;
        __builtin_amdgcn_s_setprio(0); SB;
        asm volatile("s_waitcnt lgkmcnt(0)" ::: "memory"); SB;
        MF(acc[1][0][0], W28, "a", cat2(h34_0_0, h34_0_1), "v"); SB;
        LRT2(h30_2_0, hp2, 10240); SB;
        MF(acc[1][0][1], W29, "a", cat2(h34_0_0, h34_0_1), "v"); SB;
        LRT2(h30_2_1, hp2, 10752); SB;
        MF(acc[1][1][0], W28, "a", cat2(h34_1_0, h34_1_1), "v"); SB;
        LRT2(h30_3_0, hp3, 10240); SB;
        LRD4(V10, "a", vlr, 10240); SB;
        MF(acc[1][1][1], W29, "a", cat2(h34_1_0, h34_1_1), "v"); SB;
        LRT2(h30_3_1, hp3, 10752); SB;
        __builtin_amdgcn_s_setprio(0); SB;
        asm volatile("s_waitcnt lgkmcnt(0)" ::: "memory"); SB;
        MF(acc[1][2][0], W20, "a", cat2(h30_2_0, h30_2_1), "v"); SB;
        LRT2(h35_0_0, hp0, 15360); SB;
        MF(acc[1][2][1], W21, "a", cat2(h30_2_0, h30_2_1), "v"); SB;
        LRT2(h35_0_1, hp0, 15872); SB;
        MF(acc[1][3][0], W20, "a", cat2(h30_3_0, h30_3_1), "v"); SB;
        LRT2(h35_1_0, hp1, 15360); SB;
        LRD4(V11, "a", vlr, 11264); SB;
        MF(acc[1][3][1], W21, "a", cat2(h30_3_0, h30_3_1), "v"); SB;
        LRT2(h35_1_1, hp1, 15872); SB;
        BAR(); SB;
        __builtin_amdgcn_s_setprio(3); SB;
        asm volatile("s_waitcnt lgkmcnt(0)" ::: "memory"); SB;
        MF(acc[1][0][0], W30, "a", cat2(h35_0_0, h35_0_1), "v"); SB;
        LRT2(h31_2_0, hp2, 11264); SB;
        MF(acc[1][0][1], W31, "a", cat2(h35_0_0, h35_0_1), "v"); SB;
        LRT2(h31_2_1, hp2, 11776); SB;
        MF(acc[1][1][0], W30, "a", cat2(h35_1_0, h35_1_1), "v"); SB;
        LRT2(h31_3_0, hp3, 11264); SB;
        GLD4(V12, "a", vo16, vsrc + 12288, 0, " sc1"); SB;
        MF(acc[1][1][1], W31, "a", cat2(h35_1_0, h35_1_1), "v"); SB;
        LRT2(h31_3_1, hp3, 11776); SB;
        __builtin_amdgcn_s_setprio(2); SB;
        asm volatile("s_waitcnt lgkmcnt(0)" ::: "memory"); SB;
        MF(acc[1][2][0], W22, "a", cat2(h31_2_0, h31_2_1), "v"); SB;
        LRT2(h36_0_0, hp0, 16384); SB;
        MF(acc[1][2][1], W23, "a", cat2(h31_2_0, h31_2_1), "v"); SB;
        LRT2(h36_0_1, hp0, 16896); SB;
        MF(acc[1][3][0], W22, "a", cat2(h31_3_0, h31_3_1), "v"); SB;
        LRT2(h36_1_0, hp1, 16384); SB;
        GLD4(V13, "a", vo16, vsrc + 12288, 1024, " sc1"); SB;
        MF(acc[1][3][1], W23, "a", cat2(h31_3_0, h31_3_1), "v"); SB;
        LRT2(h36_1_1, hp1, 16896); SB;
        __builtin_amdgcn_s_setprio(1); SB;
        asm volatile("s_waitcnt lgkmcnt(0)" ::: "memory"); SB;
        MF(acc[1][0][0], W32, "a", cat2(h36_0_0, h36_0_1), "v"); SB;
        LRT2(h32_2_0, hp2, 12288); SB;
        MF(acc[1][0][1], W33, "a", cat2(h36_0_0, h36_0_1), "v"); SB;
        LRT2(h32_2_1, hp2, 12800); SB;
        GLD1(fl2, voF1, flH, 0, " sc1"); SB;
        MF(acc[1][1][0], W32, "a", cat2(h36_1_0, h36_1_1), "v"); SB;
        LRT2(h32_3_0, hp3, 12288); SB;
        MF(acc[1][1][1], W33, "a", cat2(h36_1_0, h36_1_1), "v"); SB;
        LRT2(h32_3_1, hp3, 12800); SB;
        __builtin_amdgcn_s_setprio(0); SB;
        asm volatile("s_waitcnt lgkmcnt(0)" ::: "memory"); SB;
        MF(acc[1][2][0], W24, "a", cat2(h32_2_0, h32_2_1), "v"); SB;
        LRT2(h37_0_0, hp0, 17408); SB;
        GLD4(V14, "a", vo16, vsrc + 12288, 2048, " sc1"); SB;
        MF(acc[1][2][1], W25, "a", cat2(h32_2_0, h32_2_1), "v"); SB;
        LRT2(h37_0_1, hp0, 17920); SB;
        MF(acc[1][3][0], W24, "a", cat2(h32_3_0, h32_3_1), "v"); SB;
        LRT2(h37_1_0, hp1, 17408); SB;
        GLD4(V15, "a", vo16, vsrc + 12288, 3072, " sc1"); SB;
        MF(acc[1][3][1], W25, "a", cat2(h32_3_0, h32_3_1), "v"); SB;
        LRT2(h37_1_1, hp1, 17920); SB;
        __builtin_amdgcn_s_setprio(0); SB;
        asm volatile("s_waitcnt lgkmcnt(0)" ::: "memory"); SB;
        MF(acc[1][0][0], W34, "a", cat2(h37_0_0, h37_0_1), "v"); SB;
        LRT2(h33_2_0, hp2, 13312); SB;
        MF(acc[1][0][1], W35, "a", cat2(h37_0_0, h37_0_1), "v"); SB;
        LRT2(h33_2_1, hp2, 13824); SB;
        MF(acc[1][1][0], W34, "a", cat2(h37_1_0, h37_1_1), "v"); SB;
        LRT2(h33_3_0, hp3, 13312); SB;
        GLD4(V16, "a", vo16, vsrc + 16384, 0, " sc1"); SB;
        MF(acc[1][1][1], W35, "a", cat2(h37_1_0, h37_1_1), "v"); SB;
        LRT2(h33_3_1, hp3, 13824); SB;
        __builtin_amdgcn_s_setprio(0); SB;
        asm volatile("s_waitcnt lgkmcnt(0)" ::: "memory"); SB;
        MF(acc[1][2][0], W26, "a", cat2(h33_2_0, h33_2_1), "v"); SB;
        LRT2(h38_0_0, hp0, 18432); SB;
        MF(acc[1][2][1], W27, "a", cat2(h33_2_0, h33_2_1), "v"); SB;
        LRT2(h38_0_1, hp0, 18944); SB;
        MF(acc[1][3][0], W26, "a", cat2(h33_3_0, h33_3_1), "v"); SB;
        LRT2(h38_1_0, hp1, 18432); SB;
        GLD4(V17, "a", vo16, vsrc + 16384, 1024, " sc1"); SB;
        MF(acc[1][3][1], W27, "a", cat2(h33_3_0, h33_3_1), "v"); SB;
        LRT2(h38_1_1, hp1, 18944); SB;
        __builtin_amdgcn_s_setprio(0); SB;
        asm volatile("s_waitcnt lgkmcnt(0)" ::: "memory"); SB;
        MF(acc[1][0][0], W36, "a", cat2(h38_0_0, h38_0_1), "v"); SB;
        LRT2(h34_2_0, hp2, 14336); SB;
        MF(acc[1][0][1], W37, "a", cat2(h38_0_0, h38_0_1), "v"); SB;
        LRT2(h34_2_1, hp2, 14848); SB;
        MF(acc[1][1][0], W36, "a", cat2(h38_1_0, h38_1_1), "v"); SB;
        LRT2(h34_3_0, hp3, 14336); SB;
        GLD4(V18, "a", vo16, vsrc + 16384, 2048, " sc1"); SB;
        MF(acc[1][1][1], W37, "a", cat2(h38_1_0, h38_1_1), "v"); SB;
        LRT2(h34_3_1, hp3, 14848); SB;
        __builtin_amdgcn_s_setprio(0); SB;
        asm volatile("s_waitcnt lgkmcnt(0)" ::: "memory"); SB;
        MF(acc[1][2][0], W28, "a", cat2(h34_2_0, h34_2_1), "v"); SB;
        LRT2(h39_0_0, hp0, 19456); SB;
        MF(acc[1][2][1], W29, "a", cat2(h34_2_0, h34_2_1), "v"); SB;
        LRT2(h39_0_1, hp0, 19968); SB;
        MF(acc[1][3][0], W28, "a", cat2(h34_3_0, h34_3_1), "v"); SB;
        LRT2(h39_1_0, hp1, 19456); SB;
        GLD4(V19, "a", vo16, vsrc + 16384, 3072, " sc1"); SB;
        MF(acc[1][3][1], W29, "a", cat2(h34_3_0, h34_3_1), "v"); SB;
        LRT2(h39_1_1, hp1, 19968); SB;
        __builtin_amdgcn_s_setprio(0); SB;
        asm volatile("s_waitcnt lgkmcnt(0)" ::: "memory"); SB;
        MF(acc[1][0][0], W38, "a", cat2(h39_0_0, h39_0_1), "v"); SB;
        LRT2(h35_2_0, hp2, 15360); SB;
        MF(acc[1][0][1], W39, "a", cat2(h39_0_0, h39_0_1), "v"); SB;
        LRT2(h35_2_1, hp2, 15872); SB;
        GLD4(V20, "a", vo16, vsrc + 20480, 0, " sc1"); SB;
        MF(acc[1][1][0], W38, "a", cat2(h39_1_0, h39_1_1), "v"); SB;
        LRT2(h35_3_0, hp3, 15360); SB;
        MF(acc[1][1][1], W39, "a", cat2(h39_1_0, h39_1_1), "v"); SB;
        LRT2(h35_3_1, hp3, 15872); SB;
        __builtin_amdgcn_s_setprio(0); SB;
        asm volatile("s_waitcnt lgkmcnt(0)" ::: "memory"); SB;
        MF(acc[1][2][0], W30, "a", cat2(h35_2_0, h35_2_1), "v"); SB;
        LRT2(h36_2_0, hp2, 16384); SB;
        GLD4(V21, "a", vo16, vsrc + 20480, 1024, " sc1"); SB;
        MF(acc[1][2][1], W31, "a", cat2(h35_2_0, h35_2_1), "v"); SB;
        LRT2(h36_2_1, hp2, 16896); SB;
        MF(acc[1][3][0], W30, "a", cat2(h35_3_0, h35_3_1), "v"); SB;
        LRT2(h36_3_0, hp3, 16384); SB;
        asm volatile("s_waitcnt vmcnt(8)" : "+v"(fl2) :: "memory"); SB;
        CHECK1(fl2, flH, voF1); SB;
        MF(acc[1][3][1], W31, "a", cat2(h35_3_0, h35_3_1), "v"); SB;
        LRT2(h36_3_1, hp3, 16896); SB;
        __builtin_amdgcn_s_setprio(0); SB;
        asm volatile("s_waitcnt lgkmcnt(0)" ::: "memory"); SB;
        MF(acc[1][2][0], W32, "a", cat2(h36_2_0, h36_2_1), "v"); SB;
        LRT2(h37_2_0, hp2, 17408); SB;
        MF(acc[1][2][1], W33, "a", cat2(h36_2_0, h36_2_1), "v"); SB;
        LRT2(h37_2_1, hp2, 17920); SB;
        GLD4(HA0_0, "v", vo16, fsA + 0, 0, " sc1"); SB;
        MF(acc[1][3][0], W32, "a", cat2(h36_3_0, h36_3_1), "v"); SB;
        LRT2(h37_3_0, hp3, 17408); SB;
        MF(acc[1][3][1], W33, "a", cat2(h36_3_0, h36_3_1), "v"); SB;
        LRT2(h37_3_1, hp3, 17920); SB;
        GLD4(HA0_1, "v", vo16, fsA + 49152, 0, " sc1"); SB;
        BAR(); SB;
        __builtin_amdgcn_s_setprio(3); SB;
        asm volatile("s_waitcnt lgkmcnt(0)" ::: "memory"); SB;
        MF(acc[1][2][0], W34, "a", cat2(h37_2_0, h37_2_1), "v"); SB;
        LRT2(h38_2_0, hp2, 18432); SB;
        f32x2 st1_0, st1_1, st1_2, st1_3;
        LRD2(st1_0, sp0, 512); SB;
        LRD2(st1_1, sp1, 512); SB;
        LRD2(st1_2, sp2, 512); SB;
        LRD2(st1_3, sp3, 512); SB;
        MF(acc[1][2][1], W35, "a", cat2(h37_2_0, h37_2_1), "v"); SB;
        LRT2(h38_2_1, hp2, 18944); SB;
        GLD4(HA1_0, "v", vo16, fsA + 0, 1024, " sc1"); SB;
        MF(acc[1][3][0], W34, "a", cat2(h37_3_0, h37_3_1), "v"); SB;
        LRT2(h38_3_0, hp3, 18432); SB;
        asm volatile("s_waitcnt lgkmcnt(5)" ::: "memory"); SB;
        float z1_0_0a = acc[1][0][0][0], z1_0_0b = acc[1][0][0][1], z1_0_0c = acc[1][0][0][2], z1_0_0d = acc[1][0][0][3];
        EPI_A(z1_0_0a, z1_0_0c, st1_0[0], csv[0][0], csv[0][2]); EPI_A(z1_0_0b, z1_0_0d, st1_0[0], csv[0][1], csv[0][3]); SB;
        MF(acc[1][3][1], W35, "a", cat2(h37_3_0, h37_3_1), "v"); SB;
        LRT2(h38_3_1, hp3, 18944); SB;
        GLD4(HA1_1, "v", vo16, fsA + 49152, 1024, " sc1"); SB;
        EPI_B(z1_0_0a, z1_0_0c, st1_0[1]); EPI_B(z1_0_0b, z1_0_0d, st1_0[1]); SB;
        __builtin_amdgcn_s_setprio(2); SB;
        asm volatile("s_waitcnt lgkmcnt(0)" ::: "memory"); SB;
        MF(acc[1][2][0], W36, "a", cat2(h38_2_0, h38_2_1), "v"); SB;
        LRT2(h39_2_0, hp2, 19456); SB;
        EPI_C(z1_0_0a, z1_0_0b, z1_0_0c, z1_0_0d); SB;
        EPI_D(z1_0_0a, z1_0_0b, xp0, 0); SB;
        MF(acc[1][2][1], W37, "a", cat2(h38_2_0, h38_2_1), "v"); SB;
        LRT2(h39_2_1, hp2, 19968); SB;
        GLD4(HA2_0, "v", vo16, fsA + 0, 2048, " sc1"); SB;
        float z1_0_1a = acc[1][0][1][0], z1_0_1b = acc[1][0][1][1], z1_0_1c = acc[1][0][1][2], z1_0_1d = acc[1][0][1][3];
        EPI_A(z1_0_1a, z1_0_1c, st1_0[0], csv[1][0], csv[1][2]); EPI_A(z1_0_1b, z1_0_1d, st1_0[0], csv[1][1], csv[1][3]); SB;
        MF(acc[1][3][0], W36, "a", cat2(h38_3_0, h38_3_1), "v"); SB;
        LRT2(h39_3_0, hp3, 19456); SB;
        EPI_B(z1_0_1a, z1_0_1c, st1_0[1]); EPI_B(z1_0_1b, z1_0_1d, st1_0[1]); SB;
        MF(acc[1][3][1], W37, "a", cat2(h38_3_0, h38_3_1), "v"); SB;
        LRT2(h39_3_1, hp3, 19968); SB;
        GLD4(HA2_1, "v", vo16, fsA + 49152, 2048, " sc1"); SB;
        EPI_C(z1_0_1a, z1_0_1b, z1_0_1c, z1_0_1d); SB;
        EPI_D(z1_0_1a, z1_0_1b, xp0, 256); SB;
        __builtin_amdgcn_s_setprio(1); SB;
        asm volatile("s_waitcnt lgkmcnt(1)" ::: "memory"); SB;
        MF(acc[1][2][0], W38, "a", cat2(h39_2_0, h39_2_1), "v"); SB;
        float z1_1_0a = acc[1][1][0][0], z1_1_0b = acc[1][1][0][1], z1_1_0c = acc[1][1][0][2], z1_1_0d = acc[1][1][0][3];
        EPI_A(z1_1_0a, z1_1_0c, st1_1[0], csv[0][0], csv[0][2]); EPI_A(z1_1_0b, z1_1_0d, st1_1[0], csv[0][1], csv[0][3]); SB;
        MF(acc[1][2][1], W39, "a", cat2(h39_2_0, h39_2_1), "v"); SB;
        GLD4(HA3_0, "v", vo16, fsA + 0, 3072, " sc1"); SB;
        EPI_B(z1_1_0a, z1_1_0c, st1_1[1]); EPI_B(z1_1_0b, z1_1_0d, st1_1[1]); SB;
        MF(acc[1][3][0], W38, "a", cat2(h39_3_0, h39_3_1), "v"); SB;
        EPI_C(z1_1_0a, z1_1_0b, z1_1_0c, z1_1_0d); SB;
        EPI_D(z1_1_0a, z1_1_0b, xp1, 0); SB;
        MF(acc[1][3][1], W39, "a", cat2(h39_3_0, h39_3_1), "v"); SB;
        GLD4(HA3_1, "v", vo16, fsA + 49152, 3072, " sc1"); SB;
        float z1_1_1a = acc[1][1][1][0], z1_1_1b = acc[1][1][1][1], z1_1_1c = acc[1][1][1][2], z1_1_1d = acc[1][1][1][3];
        EPI_A(z1_1_1a, z1_1_1c, st1_1[0], csv[1][0], csv[1][2]); EPI_A(z1_1_1b, z1_1_1d, st1_1[0], csv[1][1], csv[1][3]); SB;
        EPI_B(z1_1_1a, z1_1_1c, st1_1[1]); EPI_B(z1_1_1b, z1_1_1d, st1_1[1]); SB;
        EPI_C(z1_1_1a, z1_1_1b, z1_1_1c, z1_1_1d); SB;
        EPI_D(z1_1_1a, z1_1_1b, xp1, 256); SB;
        STAMP(10);
        MFNOP(acc[1][3][1]); SB;
        GLD4(HA4_0, "v", vo16, fsA + 4096, 0, " sc1"); SB;
        float z1_2_0a = acc[1][2][0][0], z1_2_0b = acc[1][2][0][1], z1_2_0c = acc[1][2][0][2], z1_2_0d = acc[1][2][0][3];
        EPI_A(z1_2_0a, z1_2_0c, st1_2[0], csv[0][0], csv[0][2]); EPI_A(z1_2_0b, z1_2_0d, st1_2[0], csv[0][1], csv[0][3]); SB;
        GLD4(HA4_1, "v", vo16, fsA + 53248, 0, " sc1"); SB;
        EPI_B(z1_2_0a, z1_2_0c, st1_2[1]); EPI_B(z1_2_0b, z1_2_0d, st1_2[1]); SB;
        GLD4(HA5_0, "v", vo16, fsA + 4096, 1024, " sc1"); SB;
        EPI_C(z1_2_0a, z1_2_0b, z1_2_0c, z1_2_0d); SB;
        EPI_D(z1_2_0a, z1_2_0b, xp2, 0); SB;
        GLD4(HA5_1, "v", vo16, fsA + 53248, 1024, " sc1"); SB;
        float z1_2_1a = acc[1][2][1][0], z1_2_1b = acc[1][2][1][1], z1_2_1c = acc[1][2][1][2], z1_2_1d = acc[1][2][1][3];
        EPI_A(z1_2_1a, z1_2_1c, st1_2[0], csv[1][0], csv[1][2]); EPI_A(z1_2_1b, z1_2_1d, st1_2[0], csv[1][1], csv[1][3]); SB;
        asm volatile("s_waitcnt vmcnt(10)" ::: "memory"); SB;
        __builtin_amdgcn_s_setprio(0); SB;
        MF0(oc[0][0][0], V0, "a", HA0_0, "v"); SB;
        EPI_B(z1_2_1a, z1_2_1c, st1_2[1]); EPI_B(z1_2_1b, z1_2_1d, st1_2[1]); SB;
        MF0(oc[0][0][1], V1, "a", HA0_0, "v"); SB;
        GLD4(V22, "a", vo16, vsrc + 20480, 2048, " sc1"); SB;
        EPI_C(z1_2_1a, z1_2_1b, z1_2_1c, z1_2_1d); SB;
        EPI_D(z1_2_1a, z1_2_1b, xp2, 256); SB;
        MF0(oc[0][0][2], V2, "a", HA0_0, "v"); SB;
        float z1_3_0a = acc[1][3][0][0], z1_3_0b = acc[1][3][0][1], z1_3_0c = acc[1][3][0][2], z1_3_0d = acc[1][3][0][3];
        EPI_A(z1_3_0a, z1_3_0c, st1_3[0], csv[0][0], csv[0][2]); EPI_A(z1_3_0b, z1_3_0d, st1_3[0], csv[0][1], csv[0][3]); SB;
        MF0(oc[0][0][3], V3, "a", HA0_0, "v"); SB;
        GLD4(V23, "a", vo16, vsrc + 20480, 3072, " sc1"); SB;
        EPI_B(z1_3_0a, z1_3_0c, st1_3[1]); EPI_B(z1_3_0b, z1_3_0d, st1_3[1]); SB;
        MF0(oc[0][0][4], V4, "a", HA0_0, "v"); SB;
        EPI_C(z1_3_0a, z1_3_0b, z1_3_0c, z1_3_0d); SB;
        EPI_D(z1_3_0a, z1_3_0b, xp3, 0); SB;
        MF0(oc[0][1][0], V0, "a", HA0_1, "v"); SB;
        float z1_3_1a = acc[1][3][1][0], z1_3_1b = acc[1][3][1][1], z1_3_1c = acc[1][3][1][2], z1_3_1d = acc[1][3][1][3];
        EPI_A(z1_3_1a, z1_3_1c, st1_3[0], csv[1][0], csv[1][2]); EPI_A(z1_3_1b, z1_3_1d, st1_3[0], csv[1][1], csv[1][3]); SB;
        MF0(oc[0][1][1], V1, "a", HA0_1, "v"); SB;
        GLD4(V24, "a", vo16, vsrc + 24576, 0, " sc1"); SB;
        EPI_B(z1_3_1a, z1_3_1c, st1_3[1]); EPI_B(z1_3_1b, z1_3_1d, st1_3[1]); SB;
        MF0(oc[0][1][2], V2, "a", HA0_1, "v"); SB;
        EPI_C(z1_3_1a, z1_3_1b, z1_3_1c, z1_3_1d); SB;
        EPI_D(z1_3_1a, z1_3_1b, xp3, 256); SB;
        MF0(oc[0][1][3], V3, "a", HA0_1, "v"); SB;
        GLD4(V25, "a", vo16, vsrc + 24576, 1024, " sc1"); SB;
        MF0(oc[0][1][4], V4, "a", HA0_1, "v"); SB;
        asm volatile("s_waitcnt vmcnt(12)" ::: "memory"); SB;
        __builtin_amdgcn_s_setprio(0); SB;
        MF(oc[0][0][0], V5, "a", HA1_0, "v"); SB;
        MF(oc[0][0][1], V6, "a", HA1_0, "v"); SB;
        GLD4(V26, "a", vo16, vsrc + 24576, 2048, " sc1"); SB;
        MF(oc[0][0][2], V7, "a", HA1_0, "v"); SB;
        MF(oc[0][0][3], V8, "a", HA1_0, "v"); SB;
        GLD4(V27, "a", vo16, vsrc + 24576, 3072, " sc1"); SB;
        MF(oc[0][0][4], V9, "a", HA1_0, "v"); SB;
        MF(oc[0][1][0], V5, "a", HA1_1, "v"); SB;
        MF(oc[0][1][1], V6, "a", HA1_1, "v"); SB;
        GLD4(V28, "a", vo16, vsrc + 28672, 0, " sc1"); SB;
        MF(oc[0][1][2], V7, "a", HA1_1, "v"); SB;
        MF(oc[0][1][3], V8, "a", HA1_1, "v"); SB;
        GLD4(V29, "a", vo16, vsrc + 28672, 1024, " sc1"); SB;
        MF(oc[0][1][4], V9, "a", HA1_1, "v"); SB;
        asm volatile("s_waitcnt lgkmcnt(0)" ::: "memory"); SB;
        BAR(); SB;
        asm volatile("s_waitcnt vmcnt(14)" ::: "memory"); SB;
        __builtin_amdgcn_s_setprio(3); SB;
        MF(oc[0][0][0], V10, "a", HA2_0, "v"); SB;
        MF(oc[0][0][1], V11, "a", HA2_0, "v"); SB;
        MF(oc[0][0][2], V12, "a", HA2_0, "v"); SB;
        MF(oc[0][0][3], V13, "a", HA2_0, "v"); SB;
        MF(oc[0][0][4], V14, "a", HA2_0, "v"); SB;
        MF(oc[0][1][0], V10, "a", HA2_1, "v"); SB;
        MF(oc[0][1][1], V11, "a", HA2_1, "v"); SB;
        MF(oc[0][1][2], V12, "a", HA2_1, "v"); SB;
        MF(oc[0][1][3], V13, "a", HA2_1, "v"); SB;
        MF(oc[0][1][4], V14, "a", HA2_1, "v"); SB;
        asm volatile("s_waitcnt vmcnt(12)" ::: "memory"); SB;
        __builtin_amdgcn_s_setprio(2); SB;
        MF(oc[0][0][0], V15, "a", HA3_0, "v"); SB;
        MF(oc[0][0][1], V16, "a", HA3_0, "v"); SB;
        MF(oc[0][0][2], V17, "a", HA3_0, "v"); SB;
        MF(oc[0][0][3], V18, "a", HA3_0, "v"); SB;
        MF(oc[0][0][4], V19, "a", HA3_0, "v"); SB;
        MF(oc[0][1][0], V15, "a", HA3_1, "v"); SB;
        MF(oc[0][1][1], V16, "a", HA3_1, "v"); SB;
        MF(oc[0][1][2], V17, "a", HA3_1, "v"); SB;
        MF(oc[0][1][3], V18, "a", HA3_1, "v"); SB;
        MF(oc[0][1][4], V19, "a", HA3_1, "v"); SB;
        asm volatile("s_waitcnt vmcnt(5)" ::: "memory"); SB;
        __builtin_amdgcn_s_setprio(1); SB;
        MF(oc[0][0][0], V20, "a", HA4_0, "v"); SB;
        MF(oc[0][0][1], V21, "a", HA4_0, "v"); SB;
        MF(oc[0][0][2], V22, "a", HA4_0, "v"); SB;
        MF(oc[0][0][3], V23, "a", HA4_0, "v"); SB;
        MF(oc[0][0][4], V24, "a", HA4_0, "v"); SB;
        MF(oc[0][1][0], V20, "a", HA4_1, "v"); SB;
        MF(oc[0][1][1], V21, "a", HA4_1, "v"); SB;
        MF(oc[0][1][2], V22, "a", HA4_1, "v"); SB;
        MF(oc[0][1][3], V23, "a", HA4_1, "v"); SB;
        MF(oc[0][1][4], V24, "a", HA4_1, "v"); SB;
        asm volatile("s_waitcnt vmcnt(0)" ::: "memory"); SB;
        __builtin_amdgcn_s_setprio(0); SB;
        MF(oc[0][0][0], V25, "a", HA5_0, "v"); SB;
        MF(oc[0][0][1], V26, "a", HA5_0, "v"); SB;
        MF(oc[0][0][2], V27, "a", HA5_0, "v"); SB;
        MF(oc[0][0][3], V28, "a", HA5_0, "v"); SB;
        MF(oc[0][0][4], V29, "a", HA5_0, "v"); SB;
        MF(oc[0][1][0], V25, "a", HA5_1, "v"); SB;
        MF(oc[0][1][1], V26, "a", HA5_1, "v"); SB;
        MF(oc[0][1][2], V27, "a", HA5_1, "v"); SB;
        MF(oc[0][1][3], V28, "a", HA5_1, "v"); SB;
        MF(oc[0][1][4], V29, "a", HA5_1, "v"); SB;
        STAMP(12);
        MFNOP(oc[0][1][4]); SB;
        GLD4(HB[0], "v", vo16, fsB + 0, 0, " sc1"); SB;
        LWR4(rpw, oc[0][0][0], 0); SB;
        GLD4(HB[1], "v", vo16, fsB + 49152, 0, " sc1"); SB;
        LWR4(rpw, oc[0][0][1], 1024); SB;
        GLD4(HB[2], "v", vo16, fsB + 0, 1024, " sc1"); SB;
        LWR4(rpw, oc[0][0][2], 2048); SB;
        GLD4(HB[3], "v", vo16, fsB + 49152, 1024, " sc1"); SB;
        LWR4(rpw, oc[0][0][3], 3072); SB;
        GLD4(HB[4], "v", vo16, fsB + 0, 2048, " sc1"); SB;
        LWR4(rpw, oc[0][0][4], 4096); SB;
        GLD4(HB[5], "v", vo16, fsB + 49152, 2048, " sc1"); SB;
        LWR4(rpw, oc[0][1][0], 5120); SB;
        GLD4(HB[6], "v", vo16, fsB + 0, 3072, " sc1"); SB;
        LWR4(rpw, oc[0][1][1], 6144); SB;
        GLD4(HB[7], "v", vo16, fsB + 49152, 3072, " sc1"); SB;
        LWR4(rpw, oc[0][1][2], 7168); SB;
        GLD4(HB[8], "v", vo16, fsB + 4096, 0, " sc1"); SB;
        LWR4(rpw, oc[0][1][3], 8192); SB;
        GLD4(HB[9], "v", vo16, fsB + 53248, 0, " sc1"); SB;
        LWR4(rpw, oc[0][1][4], 9216); SB;
        GLD4(HB[10], "v", vo16, fsB + 4096, 1024, " sc1"); SB;
        GLD4(HB[11], "v", vo16, fsB + 53248, 1024, " sc1"); SB;
        asm volatile("s_waitcnt lgkmcnt(0)" ::: "memory"); SB;
        BAR(); SB;
        FINISH(0); SB;
        asm volatile("s_waitcnt vmcnt(1)" : "+v"(HB[0]), "+v"(HB[1]), "+v"(HB[2]), "+v"(HB[3]), "+v"(HB[4]), "+v"(HB[5]), "+v"(HB[6]), "+v"(HB[7]), "+v"(HB[8]), "+v"(HB[9]), "+v"(HB[10]), "+v"(HB[11]) :: "memory"); SB;
        HB_CHECK(); SB;
        STAMP(13);
        __builtin_amdgcn_s_setprio(3); SB;
        MF0(oc[1][0][0], V0, "a", HB[0], "v"); SB;
        MF0(oc[1][0][1], V1, "a", HB[0], "v"); SB;
        MF0(oc[1][0][2], V2, "a", HB[0], "v"); SB;
        MF0(oc[1][0][3], V3, "a", HB[0], "v"); SB;
        MF0(oc[1][0][4], V4, "a", HB[0], "v"); SB;
        MF0(oc[1][1][0], V0, "a", HB[1], "v"); SB;
        MF0(oc[1][1][1], V1, "a", HB[1], "v"); SB;
        MF0(oc[1][1][2], V2, "a", HB[1], "v"); SB;
        MF0(oc[1][1][3], V3, "a", HB[1], "v"); SB;
        MF0(oc[1][1][4], V4, "a", HB[1], "v"); SB;
        __builtin_amdgcn_s_setprio(2); SB;
        MF(oc[1][0][0], V5, "a", HB[2], "v"); SB;
        MF(oc[1][0][1], V6, "a", HB[2], "v"); SB;
        MF(oc[1][0][2], V7, "a", HB[2], "v"); SB;
        MF(oc[1][0][3], V8, "a", HB[2], "v"); SB;
        MF(oc[1][0][4], V9, "a", HB[2], "v"); SB;
        MF(oc[1][1][0], V5, "a", HB[3], "v"); SB;
        MF(oc[1][1][1], V6, "a", HB[3], "v"); SB;
        MF(oc[1][1][2], V7, "a", HB[3], "v"); SB;
        MF(oc[1][1][3], V8, "a", HB[3], "v"); SB;
        MF(oc[1][1][4], V9, "a", HB[3], "v"); SB;
        __builtin_amdgcn_s_setprio(1); SB;
        MF(oc[1][0][0], V10, "a", HB[4], "v"); SB;
        MF(oc[1][0][1], V11, "a", HB[4], "v"); SB;
        MF(oc[1][0][2], V12, "a", HB[4], "v"); SB;
        MF(oc[1][0][3], V13, "a", HB[4], "v"); SB;
        MF(oc[1][0][4], V14, "a", HB[4], "v"); SB;
        MF(oc[1][1][0], V10, "a", HB[5], "v"); SB;
        MF(oc[1][1][1], V11, "a", HB[5], "v"); SB;
        MF(oc[1][1][2], V12, "a", HB[5], "v"); SB;
        MF(oc[1][1][3], V13, "a", HB[5], "v"); SB;
        MF(oc[1][1][4], V14, "a", HB[5], "v"); SB;
        __builtin_amdgcn_s_setprio(0); SB;
        MF(oc[1][0][0], V15, "a", HB[6], "v"); SB;
        MF(oc[1][0][1], V16, "a", HB[6], "v"); SB;
        MF(oc[1][0][2], V17, "a", HB[6], "v"); SB;
        MF(oc[1][0][3], V18, "a", HB[6], "v"); SB;
        MF(oc[1][0][4], V19, "a", HB[6], "v"); SB;
        MF(oc[1][1][0], V15, "a", HB[7], "v"); SB;
        MF(oc[1][1][1], V16, "a", HB[7], "v"); SB;
        MF(oc[1][1][2], V17, "a", HB[7], "v"); SB;
        MF(oc[1][1][3], V18, "a", HB[7], "v"); SB;
        MF(oc[1][1][4], V19, "a", HB[7], "v"); SB;
        asm volatile("s_waitcnt lgkmcnt(0)" ::: "memory"); BAR(); SB;
        __builtin_amdgcn_s_setprio(3); SB;
        MF(oc[1][0][0], V20, "a", HB[8], "v"); SB;
        MF(oc[1][0][1], V21, "a", HB[8], "v"); SB;
        MF(oc[1][0][2], V22, "a", HB[8], "v"); SB;
        MF(oc[1][0][3], V23, "a", HB[8], "v"); SB;
        MF(oc[1][0][4], V24, "a", HB[8], "v"); SB;
        MF(oc[1][1][0], V20, "a", HB[9], "v"); SB;
        MF(oc[1][1][1], V21, "a", HB[9], "v"); SB;
        MF(oc[1][1][2], V22, "a", HB[9], "v"); SB;
        MF(oc[1][1][3], V23, "a", HB[9], "v"); SB;
        MF(oc[1][1][4], V24, "a", HB[9], "v"); SB;
        __builtin_amdgcn_s_setprio(2); SB;
        MF(oc[1][0][0], V25, "a", HB[10], "v"); SB;
        MF(oc[1][0][1], V26, "a", HB[10], "v"); SB;
        MF(oc[1][0][2], V27, "a", HB[10], "v"); SB;
        MF(oc[1][0][3], V28, "a", HB[10], "v"); SB;
        MF(oc[1][0][4], V29, "a", HB[10], "v"); SB;
        MF(oc[1][1][0], V25, "a", HB[11], "v"); SB;
        MF(oc[1][1][1], V26, "a", HB[11], "v"); SB;
        MF(oc[1][1][2], V27, "a", HB[11], "v"); SB;
        MF(oc[1][1][3], V28, "a", HB[11], "v"); SB;
        MF(oc[1][1][4], V29, "a", HB[11], "v"); SB;
        MFNOP(oc[1][1][4]); SB;
        LWR4(rpw, oc[1][0][0], 0); SB;
        LWR4(rpw, oc[1][0][1], 1024); SB;
        LWR4(rpw, oc[1][0][2], 2048); SB;
        LWR4(rpw, oc[1][0][3], 3072); SB;
        LWR4(rpw, oc[1][0][4], 4096); SB;
        LWR4(rpw, oc[1][1][0], 5120); SB;
        LWR4(rpw, oc[1][1][1], 6144); SB;
        LWR4(rpw, oc[1][1][2], 7168); SB;
        LWR4(rpw, oc[1][1][3], 8192); SB;
        LWR4(rpw, oc[1][1][4], 9216); SB;
        asm volatile("s_waitcnt lgkmcnt(0)" ::: "memory"); SB;
        BAR(); SB;
        FINISH(1); SB;

    } else {
        u32x4 W0, W1, W2, W3, W4, W5, W6, W7, W8, W9, W10, W11, W12, W13, W14, W15, W16, W17, W18, W19;
        if (cv >= 0) {
        const u16* hrow = hk + (size_t)d * S_;
        const char* xsrc = ws + WS_XT + (size_t)d * (S_ * 2);
        const char* ssrc = ws + WS_ST1 + w * 2048;
        const u32 hdst = lb + CV_H + w * CV_HS + 64, sdst = lb + CV_ST + (w >> 1) * 20480 + (w & 1) * 2048;
        const u32 vo16c = (d == D_ - 1 && lane == 63) ? vo16 - 16 : vo16;
        const u32 pR = lb + CV_HO + (w * 16 + (lane >> 3)) * 128 + (((lane ^ (lane >> 3)) & 7) * 16);
        const u32 voH = (w * 16 + (lane >> 3)) * 20480 + (lane & 7) * 16;
        const char* hTo = ws + WS_HT + (cv >> 3) * 1024 + (cv & 7) * 128;
        u32x4 hw;
        asm volatile("s_load_dwordx4 %0, %1, 0x0" : "=s"(hw) : "s"(hrow) : "memory"); SB;
        DMA_M0(hdst, 0); SB;
        DMA_GO1(vo16, (const char*)hrow + 4, 0, " nt"); SB;
        DMA_M0(hdst, 4160); SB;
        DMA_GO1(vo16, (const char*)hrow + 8, 0, " nt"); SB;
        DMA_M0(hdst, 8320); SB;
        DMA_GO1(vo16, (const char*)hrow + 12, 0, " nt"); SB;
        DMA_M0(hdst, 12480); SB;
        DMA_GO1(vo16, (const char*)hrow + 16, 0, " nt"); SB;
        DMA_M0(hdst, 0); SB;
        DMA_GO1(vo16, (const char*)hrow + 4, 1024, " nt"); SB;
        DMA_M0(hdst, 4160); SB;
        DMA_GO1(vo16, (const char*)hrow + 8, 1024, " nt"); SB;
        DMA_M0(hdst, 8320); SB;
        DMA_GO1(vo16, (const char*)hrow + 12, 1024, " nt"); SB;
        DMA_M0(hdst, 12480); SB;
        DMA_GO1(vo16, (const char*)hrow + 16, 1024, " nt"); SB;
        DMA_M0(sdst, 0); SB;
        DMA_GO1(vo16, ssrc + 0, 0, ""); SB;
        DMA_GO(vo16, ssrc + 0, 1024, ""); SB;
        DMA_M0(udst, 9280); SB;
        DMA_GO1(vo16, xsrc + 0, 0, ""); SB;
        DMA_GO(vo16, xsrc + 0, 1024, ""); SB;
        asm volatile("s_waitcnt lgkmcnt(0)" : "+s"(hw) :: "memory"); SB;
        {
            u32x2 z = {0u, 0u};
            *(LDS_AS u32x2*)(sm + w * 20480 + 4096 + lane * 8) = z;
            *(LDS_AS u32x2*)(sm + w * 20480 + CV_US + 4096 + lane * 8) = z;
            const int c = lane & 3, m = (lane >> 2) & 3;
            u32x4 ch;
            ch[0] = c == 3 ? hw[0] : 0u;
            ch[1] = c == 3 ? hw[1] : (c == 2 ? hw[0] : 0u);
            ch[2] = c == 3 ? hw[2] : (c == 2 ? hw[1] : (c == 1 ? hw[0] : 0u));
            ch[3] = c == 3 ? hw[3] : (c == 2 ? hw[2] : (c == 1 ? hw[1] : hw[0]));
            if (m != 3) { ch[0] = 0u; ch[1] = 0u; ch[2] = 0u; ch[3] = 0u; }
            if (lane < 16) *(LDS_AS u32x4*)(sm + CV_H + w * CV_HS + c * CV_CPY + m * 16) = ch;
            if (tid == 256) *(LDS_AS u32x4*)(sm + L_CNT) = (u32x4){0u, 0u, 0u, 0u};
        }
        asm volatile("s_waitcnt lgkmcnt(0)" ::: "memory"); SB;
        asm volatile("s_waitcnt vmcnt(8)" ::: "memory"); SB;
        BAR(); SB;
        DMA_M0(udst, 4640); SB;
        DMA_GO1(vo16, usrc + 0, 1024, ""); SB;
        DMA_GO(vo16, usrc + 0, 0, ""); SB;
        DMA_M0(udst, 0); SB;
        DMA_GO1(vo16, usrc + UR0_OFF + 0, 1024, ""); SB;
        DMA_GO(vo16, usrc + UR0_OFF + 0, 0, ""); SB;
        asm volatile("s_waitcnt vmcnt(4)" ::: "memory"); SB;
        BAR(); SB;
        DMA_M0(hdst, 0); SB;
        DMA_GO1(vo16, (const char*)hrow + 4, 2048, " nt"); SB;
        DMA_M0(hdst, 4160); SB;
        DMA_GO1(vo16, (const char*)hrow + 8, 2048, " nt"); SB;
        DMA_M0(hdst, 8320); SB;
        DMA_GO1(vo16, (const char*)hrow + 12, 2048, " nt"); SB;
        DMA_M0(hdst, 12480); SB;
        DMA_GO1(vo16, (const char*)hrow + 16, 2048, " nt"); SB;
        DMA_M0(sdst, 40960); SB;
        DMA_GO1(vo16, ssrc + 8192, 0, ""); SB;
        DMA_GO(vo16, ssrc + 8192, 1024, ""); SB;
        DMA_M0(udst, 9280); SB;
        DMA_GO1(vo16, xsrc + 0, 2048, ""); SB;
        DMA_GO(vo16, xsrc + 0, 3072, ""); SB;
        DMA_M0(hdst, 0); SB;
        DMA_GO1(vo16c, (const char*)hrow + 4, 3072, " nt"); SB;
        DMA_M0(hdst, 4160); SB;
        DMA_GO1(vo16c, (const char*)hrow + 8, 3072, " nt"); SB;
        DMA_M0(hdst, 8320); SB;
        DMA_GO1(vo16c, (const char*)hrow + 12, 3072, " nt"); SB;
        DMA_M0(hdst, 12480); SB;
        DMA_GO1(vo16c, (const char*)hrow + 16, 3072, " nt"); SB;
        asm volatile("s_waitcnt vmcnt(12)" ::: "memory"); SB;
        BAR(); SB;
        GLD4(W0, "a", vo16, wsrc + 0, 2048, ""); SB;
        GLD4(W1, "a", vo16, wsrc + 4096, 1024, ""); SB;
        GLD4(W2, "a", vo16, wsrc + 8192, 0, ""); SB;
        GLD4(W3, "a", vo16, wsrc + 8192, 3072, ""); SB;
        BAR(); SB;
        u32x4 ol0_0, ol0_1;
        LRD4(ol0_0, "v", pR, 0); SB;
        LRD4(ol0_1, "v", pR, 1024); SB;
        asm volatile("s_waitcnt lgkmcnt(0)" ::: "memory"); SB;
        GST4(voH, ol0_0, hTo + 0, 0); SB;
        GST4(voH, ol0_1, hTo + 163840, 0); SB;
        GLD4(W4, "a", vo16, wsrc + 12288, 2048, ""); SB;
        GLD4(W5, "a", vo16, wsrc + 16384, 1024, ""); SB;
        GLD4(W6, "a", vo16, wsrc + 20480, 0, ""); SB;
        GLD4(W7, "a", vo16, wsrc + 20480, 3072, ""); SB;
        GLD4(W8, "a", vo16, wsrc + 24576, 2048, ""); SB;
        GLD4(W9, "a", vo16, wsrc + 28672, 1024, ""); SB;
        asm volatile("s_waitcnt vmcnt(6)" ::: "memory"); SB;
        ARRIVE(0, ws + FL_A + cv * 128); SB;
        BAR(); SB;
        GLD4(W10, "a", vo16, wsrc + 32768, 0, ""); SB;
        GLD4(W11, "a", vo16, wsrc + 32768, 3072, ""); SB;
        GLD4(W12, "a", vo16, wsrc + 36864, 2048, ""); SB;
        GLD4(W13, "a", vo16, wsrc + 40960, 1024, ""); SB;
        GLD4(W14, "a", vo16, wsrc + 45056, 0, ""); SB;
        GLD4(W15, "a", vo16, wsrc + 45056, 3072, ""); SB;
        GLD4(W16, "a", vo16, wsrc + 49152, 2048, ""); SB;
        GLD4(W17, "a", vo16, wsrc + 53248, 1024, ""); SB;
        GLD4(W18, "a", vo16, wsrc + 57344, 0, ""); SB;
        GLD4(W19, "a", vo16, wsrc + 57344, 3072, ""); SB;
        BAR(); SB;
        bool mine = TRY1(ws + FL_A, voGA) || TRY1(ws + FL_A, voGA); if (mine) HADD(1); SB;
        const bool early = mine && HWAIT(1, 2); SB;
        if (early) { HDMA9(); } SB;
        BAR(); SB;
        u32x4 ol1_0, ol1_1;
        LRD4(ol1_0, "v", pR, 0); SB;
        LRD4(ol1_1, "v", pR, 1024); SB;
        asm volatile("s_waitcnt lgkmcnt(0)" ::: "memory"); SB;
        GST4(voH, ol1_0, hTo + 1310720, 0); SB;
        GST4(voH, ol1_1, hTo + 1474560, 0); SB;
        if (!early) { if (!mine) { spin1(ws + FL_A, voGA, 0); HADD(1); } (void)HWAIT(1, 1 << 22); HDMA9(); } SB;

        } else {
        if (tid == 256) *(LDS_AS u32x4*)(sm + L_CNT) = (u32x4){0u, 0u, 0u, 0u};
        GLD4(W0, "a", vo16, wsrc + 0, 2048, "");
        GLD4(W1, "a", vo16, wsrc + 4096, 1024, "");
        GLD4(W2, "a", vo16, wsrc + 8192, 0, "");
        GLD4(W3, "a", vo16, wsrc + 8192, 3072, "");
        GLD4(W4, "a", vo16, wsrc + 12288, 2048, "");
        GLD4(W5, "a", vo16, wsrc + 16384, 1024, "");
        GLD4(W6, "a", vo16, wsrc + 20480, 0, "");
        GLD4(W7, "a", vo16, wsrc + 20480, 3072, "");
        GLD4(W8, "a", vo16, wsrc + 24576, 2048, "");
        GLD4(W9, "a", vo16, wsrc + 28672, 1024, "");
        GLD4(W10, "a", vo16, wsrc + 32768, 0, "");
        GLD4(W11, "a", vo16, wsrc + 32768, 3072, "");
        GLD4(W12, "a", vo16, wsrc + 36864, 2048, "");
        GLD4(W13, "a", vo16, wsrc + 40960, 1024, "");
        GLD4(W14, "a", vo16, wsrc + 45056, 0, "");
        GLD4(W15, "a", vo16, wsrc + 45056, 3072, "");
        GLD4(W16, "a", vo16, wsrc + 49152, 2048, "");
        GLD4(W17, "a", vo16, wsrc + 53248, 1024, "");
        GLD4(W18, "a", vo16, wsrc + 57344, 0, "");
        GLD4(W19, "a", vo16, wsrc + 57344, 3072, "");
        if (pi < 60) {
            const int bb = pi, dpb = bb / 12, sg = bb % 12;
            const int dc = (dpb * 64 + lane) * 2;
            u32 Wv[4][8];
#pragma unroll
            for (int s = 0; s < 4; ++s)
#pragma unroll
                for (int j = 0; j < 8; ++j) {
                    const int f = (sg * 4 + s) * 32 + 8 * (j >> 1) + 2 * w + (j & 1);
                    Wv[s][j] = ldin((const u32*)(wd + (size_t)f * D_ + dc));
                }
            const int sw = (lane >> 2) & 1;
            LDS_AS char* ip = sm + (lane >> 3) * 1024 + (16 * w + 2 * (lane & 7)) * 16;
#pragma unroll
            for (int s = 0; s < 4; ++s) {
                u32x4 r0, r1;
#pragma unroll
                for (int j = 0; j < 4; ++j) {
                    r0[j] = __builtin_amdgcn_perm(Wv[s][2 * j + 1], Wv[s][2 * j], 0x05040100u);
                    r1[j] = __builtin_amdgcn_perm(Wv[s][2 * j + 1], Wv[s][2 * j], 0x07060302u);
                }
                *(LDS_AS u32x4*)(ip + s * 8192 + sw * 16) = r0; *(LDS_AS u32x4*)(ip + s * 8192 + (sw ^ 1) * 16) = r1;
            }
            asm volatile("s_waitcnt lgkmcnt(0)" ::: "memory"); BAR();
#pragma unroll
            for (int i = 0; i < 8; ++i) {
                const u32x4 o = *(LDS_AS u32x4*)(sm + (w * 8 + i) * 1024 + (lane ^ ((lane >> 3) & 1)) * 16);
                const int T = dpb * 8 + i;
                GST4(vo16, o, ws + WS_WD + (size_t)(((T / 5) * 48 + sg * 4 + w) * 5 + T % 5) * 1024, 0);
            }
            asm volatile("s_waitcnt vmcnt(0)" ::: "memory"); BAR();
            if (tid == 256) FLAG_SET(ws + FL_B + (160 + bb) * 128);
        }
        BAR();
        spin1(ws + FL_A, voGA, 0);
        BAR();
        HDMA9();

        }
        STAMP(6);
        u32x4 V0, V1, V2, V3, V4, V5, V6, V7, V8, V9, V10, V11, V12, V13, V14, V15, V16, V17, V18, V19, V20, V21, V22, V23, V24, V25, V26, V27, V28, V29;
        u32x4 HA0_0, HA0_1, HA1_0, HA1_1, HA2_0, HA2_1, HA3_0, HA3_1, HA4_0, HA4_1, HA5_0, HA5_1;
        u32x4 HB[12];
        u32x2 h0_0_0, h0_0_1, h0_1_0, h0_1_1, h0_2_0, h0_2_1, h0_3_0, h0_3_1, h1_0_0, h1_0_1, h1_1_0, h1_1_1, h1_2_0, h1_2_1, h1_3_0, h1_3_1, h2_0_0, h2_0_1, h2_1_0, h2_1_1, h2_2_0, h2_2_1, h2_3_0, h2_3_1, h3_0_0, h3_0_1, h3_1_0, h3_1_1, h3_2_0, h3_2_1, h3_3_0, h3_3_1, h4_0_0, h4_0_1, h4_1_0, h4_1_1, h4_2_0, h4_2_1, h4_3_0, h4_3_1, h5_0_0, h5_0_1, h5_1_0, h5_1_1, h5_2_0, h5_2_1, h5_3_0, h5_3_1, h6_0_0, h6_0_1, h6_1_0, h6_1_1, h6_2_0, h6_2_1, h6_3_0, h6_3_1, h7_0_0, h7_0_1, h7_1_0, h7_1_1, h7_2_0, h7_2_1, h7_3_0, h7_3_1, h8_0_0, h8_0_1, h8_1_0, h8_1_1, h8_2_0, h8_2_1, h8_3_0, h8_3_1, h9_0_0, h9_0_1, h9_1_0, h9_1_1, h9_2_0, h9_2_1, h9_3_0, h9_3_1, h10_0_0, h10_0_1, h10_1_0, h10_1_1, h10_2_0, h10_2_1, h10_3_0, h10_3_1, h11_0_0, h11_0_1, h11_1_0, h11_1_1, h11_2_0, h11_2_1, h11_3_0, h11_3_1, h12_0_0, h12_0_1, h12_1_0, h12_1_1, h12_2_0, h12_2_1, h12_3_0, h12_3_1, h13_0_0, h13_0_1, h13_1_0, h13_1_1, h13_2_0, h13_2_1, h13_3_0, h13_3_1, h14_0_0, h14_0_1, h14_1_0, h14_1_1, h14_2_0, h14_2_1, h14_3_0, h14_3_1, h15_0_0, h15_0_1, h15_1_0, h15_1_1, h15_2_0, h15_2_1, h15_3_0, h15_3_1, h16_0_0, h16_0_1, h16_1_0, h16_1_1, h16_2_0, h16_2_1, h16_3_0, h16_3_1, h17_0_0, h17_0_1, h17_1_0, h17_1_1, h17_2_0, h17_2_1, h17_3_0, h17_3_1, h18_0_0, h18_0_1, h18_1_0, h18_1_1, h18_2_0, h18_2_1, h18_3_0, h18_3_1, h19_0_0, h19_0_1, h19_1_0, h19_1_1, h19_2_0, h19_2_1, h19_3_0, h19_3_1, h20_0_0, h20_0_1, h20_1_0, h20_1_1, h20_2_0, h20_2_1, h20_3_0, h20_3_1, h21_0_0, h21_0_1, h21_1_0, h21_1_1, h21_2_0, h21_2_1, h21_3_0, h21_3_1, h22_0_0, h22_0_1, h22_1_0, h22_1_1, h22_2_0, h22_2_1, h22_3_0, h22_3_1, h23_0_0, h23_0_1, h23_1_0, h23_1_1, h23_2_0, h23_2_1, h23_3_0, h23_3_1, h24_0_0, h24_0_1, h24_1_0, h24_1_1, h24_2_0, h24_2_1, h24_3_0, h24_3_1, h25_0_0, h25_0_1, h25_1_0, h25_1_1, h25_2_0, h25_2_1, h25_3_0, h25_3_1, h26_0_0, h26_0_1, h26_1_0, h26_1_1, h26_2_0, h26_2_1, h26_3_0, h26_3_1, h27_0_0, h27_0_1, h27_1_0, h27_1_1, h27_2_0, h27_2_1, h27_3_0, h27_3_1, h28_0_0, h28_0_1, h28_1_0, h28_1_1, h28_2_0, h28_2_1, h28_3_0, h28_3_1, h29_0_0, h29_0_1, h29_1_0, h29_1_1, h29_2_0, h29_2_1, h29_3_0, h29_3_1, h30_0_0, h30_0_1, h30_1_0, h30_1_1, h30_2_0, h30_2_1, h30_3_0, h30_3_1, h31_0_0, h31_0_1, h31_1_0, h31_1_1, h31_2_0, h31_2_1, h31_3_0, h31_3_1, h32_0_0, h32_0_1, h32_1_0, h32_1_1, h32_2_0, h32_2_1, h32_3_0, h32_3_1, h33_0_0, h33_0_1, h33_1_0, h33_1_1, h33_2_0, h33_2_1, h33_3_0, h33_3_1, h34_0_0, h34_0_1, h34_1_0, h34_1_1, h34_2_0, h34_2_1, h34_3_0, h34_3_1, h35_0_0, h35_0_1, h35_1_0, h35_1_1, h35_2_0, h35_2_1, h35_3_0, h35_3_1, h36_0_0, h36_0_1, h36_1_0, h36_1_1, h36_2_0, h36_2_1, h36_3_0, h36_3_1, h37_0_0, h37_0_1, h37_1_0, h37_1_1, h37_2_0, h37_2_1, h37_3_0, h37_3_1, h38_0_0, h38_0_1, h38_1_0, h38_1_1, h38_2_0, h38_2_1, h38_3_0, h38_3_1, h39_0_0, h39_0_1, h39_1_0, h39_1_1, h39_2_0, h39_2_1, h39_3_0, h39_3_1;
        u32 fl2, fd_; f32x4 c0_, c1_, c2_;
        f32x4 acc[2][4][2], ssum[2], ssq[2], oc[2][2][5], csv[2];
        u32x4 ones4 = {0x3f803f80u, 0x3f803f80u, 0x3f803f80u, 0x3f803f80u}; PIN(ones4); SB;
        float vs[2][4] = {{0.f, 0.f, 0.f, 0.f}, {0.f, 0.f, 0.f, 0.f}};
        u32x4 sent = {0xffffffffu, 0xffffffffu, 0xffffffffu, 0xffffffffu};
        asm volatile("s_waitcnt vmcnt(7)" ::: "memory"); SB;
        BAR(); SB;
        STAMP(7);
        LRT2(h0_0_0, hp0, 0); SB;
        LRT2(h0_0_1, hp0, 512); SB;
        LRT2(h0_1_0, hp1, 0); SB;
        LRT2(h0_1_1, hp1, 512); SB;
        __builtin_amdgcn_s_setprio(3); SB;
        asm volatile("s_waitcnt lgkmcnt(0)" ::: "memory"); SB;
        MF0(acc[0][0][0], W0, "a", cat2(h0_0_0, h0_0_1), "v"); SB;
        LRT2(h1_0_0, hp0, 1024); SB;
        DMA_M0(gw, 8192); SB;
        DMA_GO1(vo16, hsA + 8192, 1024, " sc1"); SB;
        MF0(ssum[0], ones4, "v", cat2(h0_0_0, h0_0_1), "v"); SB;
        LRT2(h1_0_1, hp0, 1536); SB;
        MF0(ssq[0], cat2(h0_0_0, h0_0_1), "v", cat2(h0_0_0, h0_0_1), "v"); SB;
        LRT2(h1_1_0, hp1, 1024); SB;
        DMA_GO(vo16, hsA + 8192, 2048, " sc1"); SB;
        MF0(acc[0][1][0], W0, "a", cat2(h0_1_0, h0_1_1), "v"); SB;
        LRT2(h1_1_1, hp1, 1536); SB;
        GLD1(fd_, voGD, ws + FL_B, 0, " sc1"); SB;
        asm volatile("s_waitcnt vmcnt(6)" ::: "memory"); SB;
        BAR(); SB;
        __builtin_amdgcn_s_setprio(3); SB;
        asm volatile("s_waitcnt lgkmcnt(0)" ::: "memory"); SB;
        MF(acc[0][0][0], W1, "a", cat2(h1_0_0, h1_0_1), "v"); SB;
        LRT2(h2_0_0, hp0, 2048); SB;
        DMA_GO(vo16, hsA + 8192, 3072, " sc1"); SB;
        MF(ssum[0], ones4, "v", cat2(h1_0_0, h1_0_1), "v"); SB;
        LRT2(h2_0_1, hp0, 2560); SB;
        MF(ssq[0], cat2(h1_0_0, h1_0_1), "v", cat2(h1_0_0, h1_0_1), "v"); SB;
        LRT2(h2_1_0, hp1, 2048); SB;
        GLD4(c0_, "v", voC, csrc + 0, 0, ""); SB;
        MF(acc[0][1][0], W1, "a", cat2(h1_1_0, h1_1_1), "v"); SB;
        LRT2(h2_1_1, hp1, 2560); SB;
        GLD4(c1_, "v", voC, csrc + 4 * 12288, 0, ""); SB;
        __builtin_amdgcn_s_setprio(2); SB;
        asm volatile("s_waitcnt lgkmcnt(0)" ::: "memory"); SB;
        MF(acc[0][0][0], W2, "a", cat2(h2_0_0, h2_0_1), "v"); SB;
        LRT2(h3_0_0, hp0, 3072); SB;
        GLD4(c2_, "v", voC, csrc + (w < 2 ? 8 * 12288 : 0), 0, ""); SB;
        MF(ssum[0], ones4, "v", cat2(h2_0_0, h2_0_1), "v"); SB;
        LRT2(h3_0_1, hp0, 3584); SB;
        MF(ssq[0], cat2(h2_0_0, h2_0_1), "v", cat2(h2_0_0, h2_0_1), "v"); SB;
        LRT2(h3_1_0, hp1, 3072); SB;
        DMA_M0(gw, 12288); SB;
        DMA_GO1(vo16, hsA + 12288, 0, " sc1"); SB;
        MF(acc[0][1][0], W2, "a", cat2(h2_1_0, h2_1_1), "v"); SB;
        LRT2(h3_1_1, hp1, 3584); SB;
        asm volatile("s_waitcnt vmcnt(5)" : "+v"(fd_) :: "memory"); SB;
        CHECK1(fd_, ws + FL_B, voGD); SB;
        if (cv >= 0) ARRIVE(2, ws + FL_B + cv * 128); SB;
        __builtin_amdgcn_s_setprio(1); SB;
        asm volatile("s_waitcnt lgkmcnt(0)" ::: "memory"); SB;
        MF(acc[0][0][0], W3, "a", cat2(h3_0_0, h3_0_1), "v"); SB;
        LRT2(h4_0_0, hp0, 4096); SB;
        DMA_GO(vo16, hsA + 12288, 1024, " sc1"); SB;
        MF(ssum[0], ones4, "v", cat2(h3_0_0, h3_0_1), "v"); SB;
        LRT2(h4_0_1, hp0, 4608); SB;
        MF(ssq[0], cat2(h3_0_0, h3_0_1), "v", cat2(h3_0_0, h3_0_1), "v"); SB;
        LRT2(h4_1_0, hp1, 4096); SB;
        DMA_M0(vlw, 0); SB;
        DMA_GO1(vo16, vsrc0 + 0, 0, " sc1"); SB;
        MF(acc[0][1][0], W3, "a", cat2(h3_1_0, h3_1_1), "v"); SB;
        LRT2(h4_1_1, hp1, 4608); SB;
        GLD4(V0, "a", vo16, vsrc + 0, 0, " sc1"); SB;
        __builtin_amdgcn_s_setprio(0); SB;
        asm volatile("s_waitcnt lgkmcnt(0)" ::: "memory"); SB;
        MF(acc[0][0][0], W4, "a", cat2(h4_0_0, h4_0_1), "v"); SB;
        LRT2(h0_2_0, hp2, 0); SB;
        MF(ssum[0], ones4, "v", cat2(h4_0_0, h4_0_1), "v"); SB;
        LRT2(h0_2_1, hp2, 512); SB;
        DMA_M0(gw, 12288); SB;
        DMA_GO1(vo16, hsA + 12288, 2048, " sc1"); SB;
        MF(ssq[0], cat2(h4_0_0, h4_0_1), "v", cat2(h4_0_0, h4_0_1), "v"); SB;
        LRT2(h0_3_0, hp3, 0); SB;
        MF(acc[0][1][0], W4, "a", cat2(h4_1_0, h4_1_1), "v"); SB;
        LRT2(h0_3_1, hp3, 512); SB;
        __builtin_amdgcn_s_setprio(0); SB;
        asm volatile("s_waitcnt lgkmcnt(0)" ::: "memory"); SB;
        MF0(acc[0][2][0], W0, "a", cat2(h0_2_0, h0_2_1), "v"); SB;
        LRT2(h5_0_0, hp0, 5120); SB;
        LRT2(h5_0_1, hp0, 5632); SB;
        DMA_M0(vlw, 0); SB;
        DMA_GO1(vo16, vsrc0 + 0, 1024, " sc1"); SB;
        MF0(acc[0][3][0], W0, "a", cat2(h0_3_0, h0_3_1), "v"); SB;
        LRT2(h5_1_0, hp1, 5120); SB;
        LRT2(h5_1_1, hp1, 5632); SB;
        asm volatile("s_waitcnt vmcnt(6)" : "+v"(c0_), "+v"(c1_), "+v"(c2_) :: "memory"); SB;
        c0_ += c1_; if (w < 2) c0_ += c2_; if (lane < 48) LWR4(csw, c0_, 0); SB;
        asm volatile("s_waitcnt lgkmcnt(0)" ::: "memory"); SB;
        BAR(); SB;
        float cq0, cq1, cq2, cq3;
        LRD1(cq0, csr, 0); SB;
        LRD1(cq1, csr, 768); SB;
        LRD1(cq2, csr, 1536); SB;
        LRD1(cq3, csr, 2304); SB;
        __builtin_amdgcn_s_setprio(3); SB;
        MF(acc[0][0][0], W5, "a", cat2(h5_0_0, h5_0_1), "v"); SB;
        LRT2(h1_2_0, hp2, 1024); SB;
        MF(ssum[0], ones4, "v", cat2(h5_0_0, h5_0_1), "v"); SB;
        LRT2(h1_2_1, hp2, 1536); SB;
        DMA_M0(gw, 12288); SB;
        DMA_GO1(vo16, hsA + 12288, 3072, " sc1"); SB;
        MF(ssq[0], cat2(h5_0_0, h5_0_1), "v", cat2(h5_0_0, h5_0_1), "v"); SB;
        LRT2(h1_3_0, hp3, 1024); SB;
        MF(acc[0][1][0], W5, "a", cat2(h5_1_0, h5_1_1), "v"); SB;
        LRT2(h1_3_1, hp3, 1536); SB;
        GLD4(V1, "a", vo16, vsrc + 0, 1024, " sc1"); SB;
        __builtin_amdgcn_s_setprio(2); SB;
        asm volatile("s_waitcnt lgkmcnt(0)" : "+v"(cq0), "+v"(cq1), "+v"(cq2), "+v"(cq3) :: "memory"); SB;
        MF(acc[0][2][0], W1, "a", cat2(h1_2_0, h1_2_1), "v"); SB;
        LRT2(h6_0_0, hp0, 6144); SB;
        LRT2(h6_0_1, hp0, 6656); SB;
        MF(acc[0][3][0], W1, "a", cat2(h1_3_0, h1_3_1), "v"); SB;
        LRT2(h6_1_0, hp1, 6144); SB;
        LRT2(h6_1_1, hp1, 6656); SB;
        DMA_M0(vlw, 0); SB;
        DMA_GO1(vo16, vsrc0 + 0, 2048, " sc1"); SB;
        __builtin_amdgcn_s_setprio(1); SB;
        asm volatile("s_waitcnt lgkmcnt(0)" ::: "memory"); SB;
        MF(acc[0][0][0], W6, "a", cat2(h6_0_0, h6_0_1), "v"); SB;
        LRT2(h2_2_0, hp2, 2048); SB;
        MF(ssum[0], ones4, "v", cat2(h6_0_0, h6_0_1), "v"); SB;
        LRT2(h2_2_1, hp2, 2560); SB;
        DMA_M0(gw, 16384); SB;
        DMA_GO1(vo16, hsA + 16384, 0, " sc1"); SB;
        MF(ssq[0], cat2(h6_0_0, h6_0_1), "v", cat2(h6_0_0, h6_0_1), "v"); SB;
        LRT2(h2_3_0, hp3, 2048); SB;
        MF(acc[0][1][0], W6, "a", cat2(h6_1_0, h6_1_1), "v"); SB;
        LRT2(h2_3_1, hp3, 2560); SB;
        __builtin_amdgcn_s_setprio(0); SB;
        asm volatile("s_waitcnt lgkmcnt(0)" ::: "memory"); SB;
        MF(acc[0][2][0], W2, "a", cat2(h2_2_0, h2_2_1), "v"); SB;
        LRT2(h7_0_0, hp0, 7168); SB;
        LRT2(h7_0_1, hp0, 7680); SB;
        GLD4(V2, "a", vo16, vsrc + 0, 2048, " sc1"); SB;
        MF(acc[0][3][0], W2, "a", cat2(h2_3_0, h2_3_1), "v"); SB;
        LRT2(h7_1_0, hp1, 7168); SB;
        LRT2(h7_1_1, hp1, 7680); SB;
        cq0 = (cq0 + cq1) + (cq2 + cq3); if (lane < 48) LWR1(csf, cq0, 0); SB;
        asm volatile("s_waitcnt lgkmcnt(0)" ::: "memory"); SB;
        BAR(); SB;
        __builtin_amdgcn_s_setprio(3); SB;
        MF(acc[0][0][0], W7, "a", cat2(h7_0_0, h7_0_1), "v"); SB;
        LRT2(h3_2_0, hp2, 3072); SB;
        MF(ssum[0], ones4, "v", cat2(h7_0_0, h7_0_1), "v"); SB;
        LRT2(h3_2_1, hp2, 3584); SB;
        DMA_GO(vo16, hsA + 16384, 1024, " sc1"); SB;
        MF(ssq[0], cat2(h7_0_0, h7_0_1), "v", cat2(h7_0_0, h7_0_1), "v"); SB;
        LRT2(h3_3_0, hp3, 3072); SB;
        MF(acc[0][1][0], W7, "a", cat2(h7_1_0, h7_1_1), "v"); SB;
        LRT2(h3_3_1, hp3, 3584); SB;
        DMA_M0(vlw, 0); SB;
        DMA_GO1(vo16, vsrc0 + 0, 3072, " sc1"); SB;
        __builtin_amdgcn_s_setprio(2); SB;
        asm volatile("s_waitcnt lgkmcnt(0)" ::: "memory"); SB;
        MF(acc[0][2][0], W3, "a", cat2(h3_2_0, h3_2_1), "v"); SB;
        LRT2(h8_0_0, hp0, 8192); SB;
        LRT2(h8_0_1, hp0, 8704); SB;
        MF(acc[0][3][0], W3, "a", cat2(h3_3_0, h3_3_1), "v"); SB;
        LRT2(h8_1_0, hp1, 8192); SB;
        LRT2(h8_1_1, hp1, 8704); SB;
        GLD4(V3, "a", vo16, vsrc + 0, 3072, " sc1"); SB;
        __builtin_amdgcn_s_setprio(1); SB;
        asm volatile("s_waitcnt lgkmcnt(0)" ::: "memory"); SB;
        MF(acc[0][0][0], W8, "a", cat2(h8_0_0, h8_0_1), "v"); SB;
        LRT2(h4_2_0, hp2, 4096); SB;
        MF(ssum[0], ones4, "v", cat2(h8_0_0, h8_0_1), "v"); SB;
        LRT2(h4_2_1, hp2, 4608); SB;
        DMA_M0(gw, 16384); SB;
        DMA_GO1(vo16, hsA + 16384, 2048, " sc1"); SB;
        MF(ssq[0], cat2(h8_0_0, h8_0_1), "v", cat2(h8_0_0, h8_0_1), "v"); SB;
        LRT2(h4_3_0, hp3, 4096); SB;
        MF(acc[0][1][0], W8, "a", cat2(h8_1_0, h8_1_1), "v"); SB;
        LRT2(h4_3_1, hp3, 4608); SB;
        __builtin_amdgcn_s_setprio(0); SB;
        asm volatile("s_waitcnt lgkmcnt(0)" ::: "memory"); SB;
        MF(acc[0][2][0], W4, "a", cat2(h4_2_0, h4_2_1), "v"); SB;
        LRT2(h9_0_0, hp0, 9216); SB;
        LRT2(h9_0_1, hp0, 9728); SB;
        DMA_M0(vlw, 4096); SB;
        DMA_GO1(vo16, vsrc0 + 4096, 0, " sc1"); SB;
        MF(acc[0][3][0], W4, "a", cat2(h4_3_0, h4_3_1), "v"); SB;
        LRT2(h9_1_0, hp1, 9216); SB;
        LRT2(h9_1_1, hp1, 9728); SB;
        asm volatile("s_waitcnt vmcnt(14)" ::: "memory"); SB;
        BAR(); SB;
        __builtin_amdgcn_s_setprio(3); SB;
        asm volatile("s_waitcnt lgkmcnt(0)" ::: "memory"); SB;
        MF(acc[0][0][0], W9, "a", cat2(h9_0_0, h9_0_1), "v"); SB;
        LRT2(h5_2_0, hp2, 5120); SB;
        DMA_M0(gw, 16384); SB;
        DMA_GO1(vo16, hsA + 16384, 3072, " sc1"); SB;
        MF(ssum[0], ones4, "v", cat2(h9_0_0, h9_0_1), "v"); SB;
        LRT2(h5_2_1, hp2, 5632); SB;
        GLD1(fa_, voGB, ws + FL_B, 0, " sc1"); SB;
        MF(ssq[0], cat2(h9_0_0, h9_0_1), "v", cat2(h9_0_0, h9_0_1), "v"); SB;
        LRT2(h5_3_0, hp3, 5120); SB;
        MF(acc[0][1][0], W9, "a", cat2(h9_1_0, h9_1_1), "v"); SB;
        LRT2(h5_3_1, hp3, 5632); SB;
        GST4(vo16, sent, hoA + 3145728, 0); SB;
        __builtin_amdgcn_s_setprio(2); SB;
        asm volatile("s_waitcnt lgkmcnt(0)" ::: "memory"); SB;
        MF(acc[0][2][0], W5, "a", cat2(h5_2_0, h5_2_1), "v"); SB;
        LRT2(h10_0_0, hp0, 10240); SB;
        LRT2(h10_0_1, hp0, 10752); SB;
        GST4(vo16, sent, hoA + 3145728, 1024); SB;
        MF(acc[0][3][0], W5, "a", cat2(h5_3_0, h5_3_1), "v"); SB;
        LRT2(h10_1_0, hp1, 10240); SB;
        LRT2(h10_1_1, hp1, 10752); SB;
        GST4(vo16, sent, hoA + 3145728, 2048); SB;
        STAMP(8);
        __builtin_amdgcn_s_setprio(1); SB;
        asm volatile("s_waitcnt lgkmcnt(0)" ::: "memory"); SB;
        MF(acc[0][0][0], W10, "a", cat2(h10_0_0, h10_0_1), "v"); SB;
        LRT2(h6_2_0, hp2, 6144); SB;
        MF(ssum[0], ones4, "v", cat2(h10_0_0, h10_0_1), "v"); SB;
        LRT2(h6_2_1, hp2, 6656); SB;
        GLD4(V4, "a", vo16, vsrc + 4096, 0, " sc1"); SB;
        MF(ssq[0], cat2(h10_0_0, h10_0_1), "v", cat2(h10_0_0, h10_0_1), "v"); SB;
        LRT2(h6_3_0, hp3, 6144); SB;
        MF(acc[0][1][0], W10, "a", cat2(h10_1_0, h10_1_1), "v"); SB;
        LRT2(h6_3_1, hp3, 6656); SB;
        DMA_M0(vlw, 4096); SB;
        DMA_GO1(vo16, vsrc0 + 4096, 1024, " sc1"); SB;
        __builtin_amdgcn_s_setprio(0); SB;
        asm volatile("s_waitcnt lgkmcnt(0)" ::: "memory"); SB;
        MF(acc[0][2][0], W6, "a", cat2(h6_2_0, h6_2_1), "v"); SB;
        LRT2(h11_0_0, hp0, 11264); SB;
        LRT2(h11_0_1, hp0, 11776); SB;
        MF(acc[0][3][0], W6, "a", cat2(h6_3_0, h6_3_1), "v"); SB;
        LRT2(h11_1_0, hp1, 11264); SB;
        LRT2(h11_1_1, hp1, 11776); SB;
        GLD4(V5, "a", vo16, vsrc + 4096, 1024, " sc1"); SB;
        __builtin_amdgcn_s_setprio(0); SB;
        asm volatile("s_waitcnt lgkmcnt(0)" ::: "memory"); SB;
        MF(acc[0][0][0], W11, "a", cat2(h11_0_0, h11_0_1), "v"); SB;
        LRT2(h7_2_0, hp2, 7168); SB;
        MF(ssum[0], ones4, "v", cat2(h11_0_0, h11_0_1), "v"); SB;
        LRT2(h7_2_1, hp2, 7680); SB;
        DMA_GO(vo16, vsrc0 + 4096, 2048, " sc1"); SB;
        MF(ssq[0], cat2(h11_0_0, h11_0_1), "v", cat2(h11_0_0, h11_0_1), "v"); SB;
        LRT2(h7_3_0, hp3, 7168); SB;
        MF(acc[0][1][0], W11, "a", cat2(h11_1_0, h11_1_1), "v"); SB;
        LRT2(h7_3_1, hp3, 7680); SB;
        __builtin_amdgcn_s_setprio(0); SB;
        asm volatile("s_waitcnt lgkmcnt(0)" ::: "memory"); SB;
        MF(acc[0][2][0], W7, "a", cat2(h7_2_0, h7_2_1), "v"); SB;
        LRT2(h12_0_0, hp0, 12288); SB;
        LRT2(h12_0_1, hp0, 12800); SB;
        GLD4(V6, "a", vo16, vsrc + 4096, 2048, " sc1"); SB;
        MF(acc[0][3][0], W7, "a", cat2(h7_3_0, h7_3_1), "v"); SB;
        LRT2(h12_1_0, hp1, 12288); SB;
        LRT2(h12_1_1, hp1, 12800); SB;
        asm volatile("s_waitcnt vmcnt(8)" : "+v"(fa_) :: "memory"); SB;
        CHECK1(fa_, ws + FL_B, voGB); SB;
        BAR(); SB;
        __builtin_amdgcn_s_setprio(3); SB;
        asm volatile("s_waitcnt lgkmcnt(0)" ::: "memory"); SB;
        MF(acc[0][0][0], W12, "a", cat2(h12_0_0, h12_0_1), "v"); SB;
        LRT2(h8_2_0, hp2, 8192); SB;
        MF(ssum[0], ones4, "v", cat2(h12_0_0, h12_0_1), "v"); SB;
        LRT2(h8_2_1, hp2, 8704); SB;
        MF(ssq[0], cat2(h12_0_0, h12_0_1), "v", cat2(h12_0_0, h12_0_1), "v"); SB;
        LRT2(h8_3_0, hp3, 8192); SB;
        MF(acc[0][1][0], W12, "a", cat2(h12_1_0, h12_1_1), "v"); SB;
        LRT2(h8_3_1, hp3, 8704); SB;
        DMA_GO(vo16, vsrc0 + 4096, 3072, " sc1"); SB;
        __builtin_amdgcn_s_setprio(2); SB;
        asm volatile("s_waitcnt lgkmcnt(0)" ::: "memory"); SB;
        MF(acc[0][2][0], W8, "a", cat2(h8_2_0, h8_2_1), "v"); SB;
        LRT2(h13_0_0, hp0, 13312); SB;
        LRT2(h13_0_1, hp0, 13824); SB;
        MF(acc[0][3][0], W8, "a", cat2(h8_3_0, h8_3_1), "v"); SB;
        LRT2(h13_1_0, hp1, 13312); SB;
        LRT2(h13_1_1, hp1, 13824); SB;
        BAR(); SB;
        __builtin_amdgcn_s_setprio(3); SB;
        asm volatile("s_waitcnt lgkmcnt(0)" ::: "memory"); SB;
        MF(acc[0][0][0], W13, "a", cat2(h13_0_0, h13_0_1), "v"); SB;
        LRT2(h9_2_0, hp2, 9216); SB;
        MF(ssum[0], ones4, "v", cat2(h13_0_0, h13_0_1), "v"); SB;
        LRT2(h9_2_1, hp2, 9728); SB;
        GLD4(V7, "a", vo16, vsrc + 4096, 3072, " sc1"); SB;
        MF(ssq[0], cat2(h13_0_0, h13_0_1), "v", cat2(h13_0_0, h13_0_1), "v"); SB;
        LRT2(h9_3_0, hp3, 9216); SB;
        MF(acc[0][1][0], W13, "a", cat2(h13_1_0, h13_1_1), "v"); SB;
        LRT2(h9_3_1, hp3, 9728); SB;
        __builtin_amdgcn_s_setprio(2); SB;
        asm volatile("s_waitcnt lgkmcnt(0)" ::: "memory"); SB;
        MF(acc[0][2][0], W9, "a", cat2(h9_2_0, h9_2_1), "v"); SB;
        LRT2(h14_0_0, hp0, 14336); SB;
        LRT2(h14_0_1, hp0, 14848); SB;
        DMA_M0(vlw, 8192); SB;
        DMA_GO1(vo16, vsrc0 + 8192, 0, " sc1"); SB;
        MF(acc[0][3][0], W9, "a", cat2(h9_3_0, h9_3_1), "v"); SB;
        LRT2(h14_1_0, hp1, 14336); SB;
        LRT2(h14_1_1, hp1, 14848); SB;
        __builtin_amdgcn_s_setprio(1); SB;
        asm volatile("s_waitcnt lgkmcnt(0)" ::: "memory"); SB;
        MF(acc[0][0][0], W14, "a", cat2(h14_0_0, h14_0_1), "v"); SB;
        LRT2(h10_2_0, hp2, 10240); SB;
        MF(ssum[0], ones4, "v", cat2(h14_0_0, h14_0_1), "v"); SB;
        LRT2(h10_2_1, hp2, 10752); SB;
        MF(ssq[0], cat2(h14_0_0, h14_0_1), "v", cat2(h14_0_0, h14_0_1), "v"); SB;
        LRT2(h10_3_0, hp3, 10240); SB;
        MF(acc[0][1][0], W14, "a", cat2(h14_1_0, h14_1_1), "v"); SB;
        LRT2(h10_3_1, hp3, 10752); SB;
        GLD4(V8, "a", vo16, vsrc + 8192, 0, " sc1"); SB;
        __builtin_amdgcn_s_setprio(0); SB;
        asm volatile("s_waitcnt lgkmcnt(0)" ::: "memory"); SB;
        MF(acc[0][2][0], W10, "a", cat2(h10_2_0, h10_2_1), "v"); SB;
        LRT2(h15_0_0, hp0, 15360); SB;
        LRT2(h15_0_1, hp0, 15872); SB;
        MF(acc[0][3][0], W10, "a", cat2(h10_3_0, h10_3_1), "v"); SB;
        LRT2(h15_1_0, hp1, 15360); SB;
        LRT2(h15_1_1, hp1, 15872); SB;
        __builtin_amdgcn_s_setprio(0); SB;
        asm volatile("s_waitcnt lgkmcnt(0)" ::: "memory"); SB;
        MF(acc[0][0][0], W15, "a", cat2(h15_0_0, h15_0_1), "v"); SB;
        LRT2(h11_2_0, hp2, 11264); SB;
        MF(ssum[0], ones4, "v", cat2(h15_0_0, h15_0_1), "v"); SB;
        LRT2(h11_2_1, hp2, 11776); SB;
        DMA_GO(vo16, vsrc0 + 8192, 1024, " sc1"); SB;
        MF(ssq[0], cat2(h15_0_0, h15_0_1), "v", cat2(h15_0_0, h15_0_1), "v"); SB;
        LRT2(h11_3_0, hp3, 11264); SB;
        MF(acc[0][1][0], W15, "a", cat2(h15_1_0, h15_1_1), "v"); SB;
        LRT2(h11_3_1, hp3, 11776); SB;
        __builtin_amdgcn_s_setprio(0); SB;
        asm volatile("s_waitcnt lgkmcnt(0)" ::: "memory"); SB;
        MF(acc[0][2][0], W11, "a", cat2(h11_2_0, h11_2_1), "v"); SB;
        LRT2(h16_0_0, hp0, 16384); SB;
        LRT2(h16_0_1, hp0, 16896); SB;
        GLD4(V9, "a", vo16, vsrc + 8192, 1024, " sc1"); SB;
        MF(acc[0][3][0], W11, "a", cat2(h11_3_0, h11_3_1), "v"); SB;
        LRT2(h16_1_0, hp1, 16384); SB;
        LRT2(h16_1_1, hp1, 16896); SB;
        __builtin_amdgcn_s_setprio(0); SB;
        asm volatile("s_waitcnt lgkmcnt(0)" ::: "memory"); SB;
        MF(acc[0][0][0], W16, "a", cat2(h16_0_0, h16_0_1), "v"); SB;
        LRT2(h12_2_0, hp2, 12288); SB;
        MF(ssum[0], ones4, "v", cat2(h16_0_0, h16_0_1), "v"); SB;
        LRT2(h12_2_1, hp2, 12800); SB;
        DMA_M0(gw, 0); SB;
        DMA_GO1(vo16, hsB + 0, 0, " sc1"); SB;
        MF(ssq[0], cat2(h16_0_0, h16_0_1), "v", cat2(h16_0_0, h16_0_1), "v"); SB;
        LRT2(h12_3_0, hp3, 12288); SB;
        MF(acc[0][1][0], W16, "a", cat2(h16_1_0, h16_1_1), "v"); SB;
        LRT2(h12_3_1, hp3, 12800); SB;
        __builtin_amdgcn_s_setprio(0); SB;
        asm volatile("s_waitcnt lgkmcnt(0)" ::: "memory"); SB;
        MF(acc[0][2][0], W12, "a", cat2(h12_2_0, h12_2_1), "v"); SB;
        LRT2(h17_0_0, hp0, 17408); SB;
        LRT2(h17_0_1, hp0, 17920); SB;
        DMA_M0(vlw, 8192); SB;
        DMA_GO1(vo16, vsrc0 + 8192, 2048, " sc1"); SB;
        MF(acc[0][3][0], W12, "a", cat2(h12_3_0, h12_3_1), "v"); SB;
        LRT2(h17_1_0, hp1, 17408); SB;
        LRT2(h17_1_1, hp1, 17920); SB;
        BAR(); SB;
        __builtin_amdgcn_s_setprio(3); SB;
        asm volatile("s_waitcnt lgkmcnt(0)" ::: "memory"); SB;
        MF(acc[0][0][0], W17, "a", cat2(h17_0_0, h17_0_1), "v"); SB;
        LRT2(h13_2_0, hp2, 13312); SB;
        MF(ssum[0], ones4, "v", cat2(h17_0_0, h17_0_1), "v"); SB;
        LRT2(h13_2_1, hp2, 13824); SB;
        DMA_M0(gw, 0); SB;
        DMA_GO1(vo16, hsB + 0, 1024, " sc1"); SB;
        MF(ssq[0], cat2(h17_0_0, h17_0_1), "v", cat2(h17_0_0, h17_0_1), "v"); SB;
        LRT2(h13_3_0, hp3, 13312); SB;
        MF(acc[0][1][0], W17, "a", cat2(h17_1_0, h17_1_1), "v"); SB;
        LRT2(h13_3_1, hp3, 13824); SB;
        GLD4(V10, "a", vo16, vsrc + 8192, 2048, " sc1"); SB;
        __builtin_amdgcn_s_setprio(2); SB;
        asm volatile("s_waitcnt lgkmcnt(0)" ::: "memory"); SB;
        MF(acc[0][2][0], W13, "a", cat2(h13_2_0, h13_2_1), "v"); SB;
        LRT2(h18_0_0, hp0, 18432); SB;
        LRT2(h18_0_1, hp0, 18944); SB;
        MF(acc[0][3][0], W13, "a", cat2(h13_3_0, h13_3_1), "v"); SB;
        LRT2(h18_1_0, hp1, 18432); SB;
        LRT2(h18_1_1, hp1, 18944); SB;
        DMA_M0(vlw, 8192); SB;
        DMA_GO1(vo16, vsrc0 + 8192, 3072, " sc1"); SB;
        __builtin_amdgcn_s_setprio(1); SB;
        asm volatile("s_waitcnt lgkmcnt(0)" ::: "memory"); SB;
        MF(acc[0][0][0], W18, "a", cat2(h18_0_0, h18_0_1), "v"); SB;
        LRT2(h14_2_0, hp2, 14336); SB;
        MF(ssum[0], ones4, "v", cat2(h18_0_0, h18_0_1), "v"); SB;
        LRT2(h14_2_1, hp2, 14848); SB;
        DMA_M0(gw, 0); SB;
        DMA_GO1(vo16, hsB + 0, 2048, " sc1"); SB;
        MF(ssq[0], cat2(h18_0_0, h18_0_1), "v", cat2(h18_0_0, h18_0_1), "v"); SB;
        LRT2(h14_3_0, hp3, 14336); SB;
        MF(acc[0][1][0], W18, "a", cat2(h18_1_0, h18_1_1), "v"); SB;
        LRT2(h14_3_1, hp3, 14848); SB;
        __builtin_amdgcn_s_setprio(0); SB;
        asm volatile("s_waitcnt lgkmcnt(0)" ::: "memory"); SB;
        MF(acc[0][2][0], W14, "a", cat2(h14_2_0, h14_2_1), "v"); SB;
        LRT2(h19_0_0, hp0, 19456); SB;
        LRT2(h19_0_1, hp0, 19968); SB;
        GLD4(V11, "a", vo16, vsrc + 8192, 3072, " sc1"); SB;
        MF(acc[0][3][0], W14, "a", cat2(h14_3_0, h14_3_1), "v"); SB;
        LRT2(h19_1_0, hp1, 19456); SB;
        LRT2(h19_1_1, hp1, 19968); SB;
        __builtin_amdgcn_s_setprio(0); SB;
        asm volatile("s_waitcnt lgkmcnt(0)" ::: "memory"); SB;
        MF(acc[0][0][0], W19, "a", cat2(h19_0_0, h19_0_1), "v"); SB;
        LRT2(h15_2_0, hp2, 15360); SB;
        MF(ssum[0], ones4, "v", cat2(h19_0_0, h19_0_1), "v"); SB;
        LRT2(h15_2_1, hp2, 15872); SB;
        DMA_GO(vo16, hsB + 0, 3072, " sc1"); SB;
        MF(ssq[0], cat2(h19_0_0, h19_0_1), "v", cat2(h19_0_0, h19_0_1), "v"); SB;
        LRT2(h15_3_0, hp3, 15360); SB;
        MF(acc[0][1][0], W19, "a", cat2(h19_1_0, h19_1_1), "v"); SB;
        LRT2(h15_3_1, hp3, 15872); SB;
        GLD4(V12, "v", vo16, vsrc + 12288, 0, " sc1"); SB;
        __builtin_amdgcn_s_setprio(0); SB;
        asm volatile("s_waitcnt lgkmcnt(0)" ::: "memory"); SB;
        MF(acc[0][2][0], W15, "a", cat2(h15_2_0, h15_2_1), "v"); SB;
        LRT2(h16_2_0, hp2, 16384); SB;
        LRT2(h16_2_1, hp2, 16896); SB;
        MFNOP(ssq[0]); SB;
        {   float q = (a & 2) ? ((a & 1) ? ssq[0][3] : ssq[0][2]) : ((a & 1) ? ssq[0][1] : ssq[0][0]);
            f32x2 o; o[0] = ssum[0][0] * (-1.f / D_); o[1] = __builtin_amdgcn_rcpf(fmaxf(q * (1.f / D_) - o[0] * o[0], 0.f) + 1e-5f);
            if (g == (a >> 2)) LWR2(sp0, o, 0); } SB;
        MF(acc[0][3][0], W15, "a", cat2(h15_3_0, h15_3_1), "v"); SB;
        LRT2(h16_3_0, hp3, 16384); SB;
        LRT2(h16_3_1, hp3, 16896); SB;
        GLD4(V13, "v", vo16, vsrc + 12288, 1024, " sc1"); SB;
        STAMP(9);
        __builtin_amdgcn_s_setprio(0); SB;
        asm volatile("s_waitcnt lgkmcnt(0)" ::: "memory"); SB;
        MF(acc[0][2][0], W16, "a", cat2(h16_2_0, h16_2_1), "v"); SB;
        LRT2(h17_2_0, hp2, 17408); SB;
        LRT2(h17_2_1, hp2, 17920); SB;
        DMA_M0(gw, 4096); SB;
        DMA_GO1(vo16, hsB + 4096, 0, " sc1"); SB;
        MF(acc[0][3][0], W16, "a", cat2(h16_3_0, h16_3_1), "v"); SB;
        LRT2(h17_3_0, hp3, 17408); SB;
        LRT2(h17_3_1, hp3, 17920); SB;
        GLD4(V14, "v", vo16, vsrc + 12288, 2048, " sc1"); SB;
        BAR(); SB;
        __builtin_amdgcn_s_setprio(3); SB;
        asm volatile("s_waitcnt lgkmcnt(0)" ::: "memory"); SB;
        MF(acc[0][2][0], W17, "a", cat2(h17_2_0, h17_2_1), "v"); SB;
        LRT2(h18_2_0, hp2, 18432); SB;
        LRT2(h18_2_1, hp2, 18944); SB;
        DMA_GO(vo16, hsB + 4096, 1024, " sc1"); SB;
        f32x2 st0_0, st0_1, st0_2, st0_3;
        LRD2(st0_0, sp0, 0); SB;
        LRD2(st0_1, sp1, 0); SB;
        LRD2(st0_2, sp2, 0); SB;
        LRD2(st0_3, sp3, 0); SB;
        LRD4(csv[0], "v", cp, 128); SB;
        MF(acc[0][3][0], W17, "a", cat2(h17_3_0, h17_3_1), "v"); SB;
        LRT2(h18_3_0, hp3, 18432); SB;
        LRT2(h18_3_1, hp3, 18944); SB;
        GLD4(V15, "v", vo16, vsrc + 12288, 3072, " sc1"); SB;
        __builtin_amdgcn_s_setprio(2); SB;
        asm volatile("s_waitcnt lgkmcnt(0)" ::: "memory"); SB;
        MF(acc[0][2][0], W18, "a", cat2(h18_2_0, h18_2_1), "v"); SB;
        LRT2(h19_2_0, hp2, 19456); SB;
        LRT2(h19_2_1, hp2, 19968); SB;
        float z0_0_0a = acc[0][0][0][0], z0_0_0b = acc[0][0][0][1], z0_0_0c = acc[0][0][0][2], z0_0_0d = acc[0][0][0][3];
        EPI_A(z0_0_0a, z0_0_0c, st0_0[0], csv[0][0], csv[0][2]); EPI_A(z0_0_0b, z0_0_0d, st0_0[0], csv[0][1], csv[0][3]); SB;
        MF(acc[0][3][0], W18, "a", cat2(h18_3_0, h18_3_1), "v"); SB;
        LRT2(h19_3_0, hp3, 19456); SB;
        LRT2(h19_3_1, hp3, 19968); SB;
        DMA_GO(vo16, hsB + 4096, 2048, " sc1"); SB;
        EPI_B(z0_0_0a, z0_0_0c, st0_0[1]); EPI_B(z0_0_0b, z0_0_0d, st0_0[1]); SB;
        asm volatile("s_waitcnt vmcnt(7)" ::: "memory"); SB;
        BAR(); SB;
        __builtin_amdgcn_s_setprio(3); SB;
        asm volatile("s_waitcnt lgkmcnt(0)" ::: "memory"); SB;
        MF(acc[0][2][0], W19, "a", cat2(h19_2_0, h19_2_1), "v"); SB;
        LRT2(h20_0_0, hp0, 0); SB;
        LRT2(h20_0_1, hp0, 512); SB;
        EPI_C(z0_0_0a, z0_0_0b, z0_0_0c, z0_0_0d); SB;
        EPI_D(z0_0_0a, z0_0_0b, xp0, 512); SB;
        MF(acc[0][3][0], W19, "a", cat2(h19_3_0, h19_3_1), "v"); SB;
        LRT2(h20_1_0, hp1, 0); SB;
        LRT2(h20_1_1, hp1, 512); SB;
        DMA_GO(vo16, hsB + 4096, 3072, " sc1"); SB;
        float z0_1_0a = acc[0][1][0][0], z0_1_0b = acc[0][1][0][1], z0_1_0c = acc[0][1][0][2], z0_1_0d = acc[0][1][0][3];
        EPI_A(z0_1_0a, z0_1_0c, st0_1[0], csv[0][0], csv[0][2]); EPI_A(z0_1_0b, z0_1_0d, st0_1[0], csv[0][1], csv[0][3]); SB;
        EPI_B(z0_1_0a, z0_1_0c, st0_1[1]); EPI_B(z0_1_0b, z0_1_0d, st0_1[1]); SB;
        EPI_C(z0_1_0a, z0_1_0b, z0_1_0c, z0_1_0d); SB;
        EPI_D(z0_1_0a, z0_1_0b, xp1, 512); SB;
        __builtin_amdgcn_s_setprio(2); SB;
        asm volatile("s_waitcnt lgkmcnt(1)" ::: "memory"); SB;
        MF0(acc[1][0][0], W0, "a", cat2(h20_0_0, h20_0_1), "v"); SB;
        LRT2(h21_0_0, hp0, 1024); SB;
        DMA_M0(gw, 8192); SB;
        DMA_GO1(vo16, hsB + 8192, 0, " sc1"); SB;
        MF0(ssum[1], ones4, "v", cat2(h20_0_0, h20_0_1), "v"); SB;
        LRT2(h21_0_1, hp0, 1536); SB;
        float z0_2_0a = acc[0][2][0][0], z0_2_0b = acc[0][2][0][1], z0_2_0c = acc[0][2][0][2], z0_2_0d = acc[0][2][0][3];
        EPI_A(z0_2_0a, z0_2_0c, st0_2[0], csv[0][0], csv[0][2]); EPI_A(z0_2_0b, z0_2_0d, st0_2[0], csv[0][1], csv[0][3]); SB;
        MF0(ssq[1], cat2(h20_0_0, h20_0_1), "v", cat2(h20_0_0, h20_0_1), "v"); SB;
        LRT2(h21_1_0, hp1, 1024); SB;
        DMA_M0(gw3, 0); SB;
        DMA_GO1(vo16, rsrc0 + 0, 0, " sc1"); SB;
        EPI_B(z0_2_0a, z0_2_0c, st0_2[1]); EPI_B(z0_2_0b, z0_2_0d, st0_2[1]); SB;
        MF0(acc[1][1][0], W0, "a", cat2(h20_1_0, h20_1_1), "v"); SB;
        LRT2(h21_1_1, hp1, 1536); SB;
        DMA_M0(gw3, 1024); SB;
        DMA_GO1(vo16, rsrc1 + 0, 0, " sc1"); SB;
        EPI_C(z0_2_0a, z0_2_0b, z0_2_0c, z0_2_0d); SB;
        EPI_D(z0_2_0a, z0_2_0b, xp2, 512); SB;
        __builtin_amdgcn_s_setprio(1); SB;
        asm volatile("s_waitcnt lgkmcnt(1)" ::: "memory"); SB;
        MF(acc[1][0][0], W1, "a", cat2(h21_0_0, h21_0_1), "v"); SB;
        LRT2(h22_0_0, hp0, 2048); SB;
        float z0_3_0a = acc[0][3][0][0], z0_3_0b = acc[0][3][0][1], z0_3_0c = acc[0][3][0][2], z0_3_0d = acc[0][3][0][3];
        EPI_A(z0_3_0a, z0_3_0c, st0_3[0], csv[0][0], csv[0][2]); EPI_A(z0_3_0b, z0_3_0d, st0_3[0], csv[0][1], csv[0][3]); SB;
        MF(ssum[1], ones4, "v", cat2(h21_0_0, h21_0_1), "v"); SB;
        LRT2(h22_0_1, hp0, 2560); SB;
        DMA_M0(gw3, 2048); SB;
        DMA_GO1(vo16, rsrc2 + 0, 0, " sc1"); SB;
        EPI_B(z0_3_0a, z0_3_0c, st0_3[1]); EPI_B(z0_3_0b, z0_3_0d, st0_3[1]); SB;
        MF(ssq[1], cat2(h21_0_0, h21_0_1), "v", cat2(h21_0_0, h21_0_1), "v"); SB;
        LRT2(h22_1_0, hp1, 2048); SB;
        EPI_C(z0_3_0a, z0_3_0b, z0_3_0c, z0_3_0d); SB;
        EPI_D(z0_3_0a, z0_3_0b, xp3, 512); SB;
        MF(acc[1][1][0], W1, "a", cat2(h21_1_0, h21_1_1), "v"); SB;
        LRT2(h22_1_1, hp1, 2560); SB;
        DMA_M0(gw, 8192); SB;
        DMA_GO1(vo16, hsB + 8192, 1024, " sc1"); SB;
        __builtin_amdgcn_s_setprio(0); SB;
        asm volatile("s_waitcnt lgkmcnt(0)" ::: "memory"); SB;
        MF(acc[1][0][0], W2, "a", cat2(h22_0_0, h22_0_1), "v"); SB;
        LRT2(h23_0_0, hp0, 3072); SB;
        MF(ssum[1], ones4, "v", cat2(h22_0_0, h22_0_1), "v"); SB;
        LRT2(h23_0_1, hp0, 3584); SB;
        MF(ssq[1], cat2(h22_0_0, h22_0_1), "v", cat2(h22_0_0, h22_0_1), "v"); SB;
        LRT2(h23_1_0, hp1, 3072); SB;
        DMA_GO(vo16, hsB + 8192, 2048, " sc1"); SB;
        MF(acc[1][1][0], W2, "a", cat2(h22_1_0, h22_1_1), "v"); SB;
        LRT2(h23_1_1, hp1, 3584); SB;
        asm volatile("s_waitcnt vmcnt(6)" ::: "memory"); SB;
        BAR(); SB;
        __builtin_amdgcn_s_setprio(3); SB;
        asm volatile("s_waitcnt lgkmcnt(0)" ::: "memory"); SB;
        MF(acc[1][0][0], W3, "a", cat2(h23_0_0, h23_0_1), "v"); SB;
        LRT2(h24_0_0, hp0, 4096); SB;
        MF(ssum[1], ones4, "v", cat2(h23_0_0, h23_0_1), "v"); SB;
        LRT2(h24_0_1, hp0, 4608); SB;
        MF(ssq[1], cat2(h23_0_0, h23_0_1), "v", cat2(h23_0_0, h23_0_1), "v"); SB;
        LRT2(h24_1_0, hp1, 4096); SB;
        DMA_GO(vo16, hsB + 8192, 3072, " sc1"); SB;
        MF(acc[1][1][0], W3, "a", cat2(h23_1_0, h23_1_1), "v"); SB;
        LRT2(h24_1_1, hp1, 4608); SB;
        STAMP(11);
        BAR(); SB;
        __builtin_amdgcn_s_setprio(3); SB;
        asm volatile("s_waitcnt lgkmcnt(0)" ::: "memory"); SB;
        MF(acc[1][0][0], W4, "a", cat2(h24_0_0, h24_0_1), "v"); SB;
        LRT2(h20_2_0, hp2, 0); SB;
        u32x2 ho0_0, ho0_1, ho0_2, ho0_3, ho0_4, ho0_5;
        LRD2S(ho0_0, xr, 0, 1); SB;
        LRD2S(ho0_1, xr, 2, 3); SB;
        LRD2S(ho0_2, xr, 4, 5); SB;
        LRD2S(ho0_3, xr, 6, 7); SB;
        LRD2S(ho0_4, xr, 8, 9); SB;
        LRD2S(ho0_5, xr, 10, 11); SB;
        MF(ssum[1], ones4, "v", cat2(h24_0_0, h24_0_1), "v"); SB;
        LRT2(h20_2_1, hp2, 512); SB;
        MF(ssq[1], cat2(h24_0_0, h24_0_1), "v", cat2(h24_0_0, h24_0_1), "v"); SB;
        LRT2(h20_3_0, hp3, 0); SB;
        MF(acc[1][1][0], W4, "a", cat2(h24_1_0, h24_1_1), "v"); SB;
        LRT2(h20_3_1, hp3, 512); SB;
        DMA_M0(gw, 12288); SB;
        DMA_GO1(vo16, hsB + 12288, 0, " sc1"); SB;
        asm volatile("s_waitcnt lgkmcnt(3)" ::: "memory"); SB;
        GST4(vo16, cat2(ho0_0, ho0_1), hoA + 0, 0); SB;
        GST4(vo16, cat2(ho0_2, ho0_3), hoA + 0, 1024); SB;
        GST4(vo16, cat2(ho0_4, ho0_5), hoA + 0, 2048); SB;
        __builtin_amdgcn_s_setprio(2); SB;
        asm volatile("s_waitcnt lgkmcnt(0)" ::: "memory"); SB;
        MF0(acc[1][2][0], W0, "a", cat2(h20_2_0, h20_2_1), "v"); SB;
        LRT2(h25_0_0, hp0, 5120); SB;
        LRT2(h25_0_1, hp0, 5632); SB;
        MF0(acc[1][3][0], W0, "a", cat2(h20_3_0, h20_3_1), "v"); SB;
        LRT2(h25_1_0, hp1, 5120); SB;
        LRT2(h25_1_1, hp1, 5632); SB;
        __builtin_amdgcn_s_setprio(1); SB;
        asm volatile("s_waitcnt lgkmcnt(0)" ::: "memory"); SB;
        MF(acc[1][0][0], W5, "a", cat2(h25_0_0, h25_0_1), "v"); SB;
        LRT2(h21_2_0, hp2, 1024); SB;
        MF(ssum[1], ones4, "v", cat2(h25_0_0, h25_0_1), "v"); SB;
        LRT2(h21_2_1, hp2, 1536); SB;
        DMA_GO(vo16, hsB + 12288, 1024, " sc1"); SB;
        MF(ssq[1], cat2(h25_0_0, h25_0_1), "v", cat2(h25_0_0, h25_0_1), "v"); SB;
        LRT2(h21_3_0, hp3, 1024); SB;
        MF(acc[1][1][0], W5, "a", cat2(h25_1_0, h25_1_1), "v"); SB;
        LRT2(h21_3_1, hp3, 1536); SB;
        __builtin_amdgcn_s_setprio(0); SB;
        asm volatile("s_waitcnt lgkmcnt(0)" ::: "memory"); SB;
        MF(acc[1][2][0], W1, "a", cat2(h21_2_0, h21_2_1), "v"); SB;
        LRT2(h26_0_0, hp0, 6144); SB;
        LRT2(h26_0_1, hp0, 6656); SB;
        GLD4(V16, "a", vo16, vsrc + 16384, 0, " sc1"); SB;
        MF(acc[1][3][0], W1, "a", cat2(h21_3_0, h21_3_1), "v"); SB;
        LRT2(h26_1_0, hp1, 6144); SB;
        LRT2(h26_1_1, hp1, 6656); SB;
        __builtin_amdgcn_s_setprio(0); SB;
        asm volatile("s_waitcnt lgkmcnt(0)" ::: "memory"); SB;
        MF(acc[1][0][0], W6, "a", cat2(h26_0_0, h26_0_1), "v"); SB;
        LRT2(h22_2_0, hp2, 2048); SB;
        MF(ssum[1], ones4, "v", cat2(h26_0_0, h26_0_1), "v"); SB;
        LRT2(h22_2_1, hp2, 2560); SB;
        DMA_GO(vo16, hsB + 12288, 2048, " sc1"); SB;
        MF(ssq[1], cat2(h26_0_0, h26_0_1), "v", cat2(h26_0_0, h26_0_1), "v"); SB;
        LRT2(h22_3_0, hp3, 2048); SB;
        MF(acc[1][1][0], W6, "a", cat2(h26_1_0, h26_1_1), "v"); SB;
        LRT2(h22_3_1, hp3, 2560); SB;
        __builtin_amdgcn_s_setprio(0); SB;
        asm volatile("s_waitcnt lgkmcnt(0)" ::: "memory"); SB;
        MF(acc[1][2][0], W2, "a", cat2(h22_2_0, h22_2_1), "v"); SB;
        LRT2(h27_0_0, hp0, 7168); SB;
        LRT2(h27_0_1, hp0, 7680); SB;
        GLD4(V17, "a", vo16, vsrc + 16384, 1024, " sc1"); SB;
        MF(acc[1][3][0], W2, "a", cat2(h22_3_0, h22_3_1), "v"); SB;
        LRT2(h27_1_0, hp1, 7168); SB;
        LRT2(h27_1_1, hp1, 7680); SB;
        asm volatile("s_waitcnt vmcnt(8)" ::: "memory"); SB;
        BAR(); SB;
        __builtin_amdgcn_s_setprio(3); SB;
        asm volatile("s_waitcnt lgkmcnt(0)" ::: "memory"); SB;
        MF(acc[1][0][0], W7, "a", cat2(h27_0_0, h27_0_1), "v"); SB;
        LRT2(h23_2_0, hp2, 3072); SB;
        MF(ssum[1], ones4, "v", cat2(h27_0_0, h27_0_1), "v"); SB;
        LRT2(h23_2_1, hp2, 3584); SB;
        DMA_GO(vo16, hsB + 12288, 3072, " sc1"); SB;
        MF(ssq[1], cat2(h27_0_0, h27_0_1), "v", cat2(h27_0_0, h27_0_1), "v"); SB;
        LRT2(h23_3_0, hp3, 3072); SB;
        MF(acc[1][1][0], W7, "a", cat2(h27_1_0, h27_1_1), "v"); SB;
        LRT2(h23_3_1, hp3, 3584); SB;
        __builtin_amdgcn_s_setprio(2); SB;
        asm volatile("s_waitcnt lgkmcnt(0)" ::: "memory"); SB;
        MF(acc[1][2][0], W3, "a", cat2(h23_2_0, h23_2_1), "v"); SB;
        LRT2(h28_0_0, hp0, 8192); SB;
        LRT2(h28_0_1, hp0, 8704); SB;
        GLD4(V18, "a", vo16, vsrc + 16384, 2048, " sc1"); SB;
        MF(acc[1][3][0], W3, "a", cat2(h23_3_0, h23_3_1), "v"); SB;
        LRT2(h28_1_0, hp1, 8192); SB;
        LRT2(h28_1_1, hp1, 8704); SB;
        __builtin_amdgcn_s_setprio(1); SB;
        asm volatile("s_waitcnt lgkmcnt(0)" ::: "memory"); SB;
        MF(acc[1][0][0], W8, "a", cat2(h28_0_0, h28_0_1), "v"); SB;
        LRT2(h24_2_0, hp2, 4096); SB;
        MF(ssum[1], ones4, "v", cat2(h28_0_0, h28_0_1), "v"); SB;
        LRT2(h24_2_1, hp2, 4608); SB;
        DMA_M0(gw, 16384); SB;
        DMA_GO1(vo16, hsB + 16384, 0, " sc1"); SB;
        MF(ssq[1], cat2(h28_0_0, h28_0_1), "v", cat2(h28_0_0, h28_0_1), "v"); SB;
        LRT2(h24_3_0, hp3, 4096); SB;
        MF(acc[1][1][0], W8, "a", cat2(h28_1_0, h28_1_1), "v"); SB;
        LRT2(h24_3_1, hp3, 4608); SB;
        __builtin_amdgcn_s_setprio(0); SB;
        asm volatile("s_waitcnt lgkmcnt(0)" ::: "memory"); SB;
        MF(acc[1][2][0], W4, "a", cat2(h24_2_0, h24_2_1), "v"); SB;
        LRT2(h29_0_0, hp0, 9216); SB;
        LRT2(h29_0_1, hp0, 9728); SB;
        GLD4(V19, "a", vo16, vsrc + 16384, 3072, " sc1"); SB;
        MF(acc[1][3][0], W4, "a", cat2(h24_3_0, h24_3_1), "v"); SB;
        LRT2(h29_1_0, hp1, 9216); SB;
        LRT2(h29_1_1, hp1, 9728); SB;
        asm volatile("s_waitcnt vmcnt(8)" ::: "memory"); SB;
        ARRIVE(3, flH + nb * 128); SB;
        __builtin_amdgcn_s_setprio(0); SB;
        MF(acc[1][0][0], W9, "a", cat2(h29_0_0, h29_0_1), "v"); SB;
        LRT2(h25_2_0, hp2, 5120); SB;
        MF(ssum[1], ones4, "v", cat2(h29_0_0, h29_0_1), "v"); SB;
        LRT2(h25_2_1, hp2, 5632); SB;
        DMA_GO(vo16, hsB + 16384, 1024, " sc1"); SB;
        MF(ssq[1], cat2(h29_0_0, h29_0_1), "v", cat2(h29_0_0, h29_0_1), "v"); SB;
        LRT2(h25_3_0, hp3, 5120); SB;
        MF(acc[1][1][0], W9, "a", cat2(h29_1_0, h29_1_1), "v"); SB;
        LRT2(h25_3_1, hp3, 5632); SB;
        __builtin_amdgcn_s_setprio(0); SB;
        asm volatile("s_waitcnt lgkmcnt(0)" ::: "memory"); SB;
        MF(acc[1][2][0], W5, "a", cat2(h25_2_0, h25_2_1), "v"); SB;
        LRT2(h30_0_0, hp0, 10240); SB;
        LRT2(h30_0_1, hp0, 10752); SB;
        GLD4(V20, "a", vo16, vsrc + 20480, 0, " sc1"); SB;
        MF(acc[1][3][0], W5, "a", cat2(h25_3_0, h25_3_1), "v"); SB;
        LRT2(h30_1_0, hp1, 10240); SB;
        LRT2(h30_1_1, hp1, 10752); SB;
        __builtin_amdgcn_s_setprio(0); SB;
        asm volatile("s_waitcnt lgkmcnt(0)" ::: "memory"); SB;
        MF(acc[1][0][0], W10, "a", cat2(h30_0_0, h30_0_1), "v"); SB;
        LRT2(h26_2_0, hp2, 6144); SB;
        MF(ssum[1], ones4, "v", cat2(h30_0_0, h30_0_1), "v"); SB;
        LRT2(h26_2_1, hp2, 6656); SB;
        DMA_GO(vo16, hsB + 16384, 2048, " sc1"); SB;
        MF(ssq[1], cat2(h30_0_0, h30_0_1), "v", cat2(h30_0_0, h30_0_1), "v"); SB;
        LRT2(h26_3_0, hp3, 6144); SB;
        MF(acc[1][1][0], W10, "a", cat2(h30_1_0, h30_1_1), "v"); SB;
        LRT2(h26_3_1, hp3, 6656); SB;
        __builtin_amdgcn_s_setprio(0); SB;
        asm volatile("s_waitcnt lgkmcnt(0)" ::: "memory"); SB;
        MF(acc[1][2][0], W6, "a", cat2(h26_2_0, h26_2_1), "v"); SB;
        LRT2(h31_0_0, hp0, 11264); SB;
        LRT2(h31_0_1, hp0, 11776); SB;
        GLD4(V21, "a", vo16, vsrc + 20480, 1024, " sc1"); SB;
        MF(acc[1][3][0], W6, "a", cat2(h26_3_0, h26_3_1), "v"); SB;
        LRT2(h31_1_0, hp1, 11264); SB;
        LRT2(h31_1_1, hp1, 11776); SB;
        asm volatile("s_waitcnt vmcnt(7)" ::: "memory"); SB;
        BAR(); SB;
        __builtin_amdgcn_s_setprio(3); SB;
        asm volatile("s_waitcnt lgkmcnt(0)" ::: "memory"); SB;
        MF(acc[1][0][0], W11, "a", cat2(h31_0_0, h31_0_1), "v"); SB;
        LRT2(h27_2_0, hp2, 7168); SB;
        MF(ssum[1], ones4, "v", cat2(h31_0_0, h31_0_1), "v"); SB;
        LRT2(h27_2_1, hp2, 7680); SB;
        DMA_GO(vo16, hsB + 16384, 3072, " sc1"); SB;
        MF(ssq[1], cat2(h31_0_0, h31_0_1), "v", cat2(h31_0_0, h31_0_1), "v"); SB;
        LRT2(h27_3_0, hp3, 7168); SB;
        MF(acc[1][1][0], W11, "a", cat2(h31_1_0, h31_1_1), "v"); SB;
        LRT2(h27_3_1, hp3, 7680); SB;
        __builtin_amdgcn_s_setprio(2); SB;
        asm volatile("s_waitcnt lgkmcnt(0)" ::: "memory"); SB;
        MF(acc[1][2][0], W7, "a", cat2(h27_2_0, h27_2_1), "v"); SB;
        LRT2(h32_0_0, hp0, 12288); SB;
        LRT2(h32_0_1, hp0, 12800); SB;
        GLD4(V22, "a", vo16, vsrc + 20480, 2048, " sc1"); SB;
        MF(acc[1][3][0], W7, "a", cat2(h27_3_0, h27_3_1), "v"); SB;
        LRT2(h32_1_0, hp1, 12288); SB;
        LRT2(h32_1_1, hp1, 12800); SB;
        __builtin_amdgcn_s_setprio(1); SB;
        asm volatile("s_waitcnt lgkmcnt(0)" ::: "memory"); SB;
        MF(acc[1][0][0], W12, "a", cat2(h32_0_0, h32_0_1), "v"); SB;
        LRT2(h28_2_0, hp2, 8192); SB;
        MF(ssum[1], ones4, "v", cat2(h32_0_0, h32_0_1), "v"); SB;
        LRT2(h28_2_1, hp2, 8704); SB;
        MF(ssq[1], cat2(h32_0_0, h32_0_1), "v", cat2(h32_0_0, h32_0_1), "v"); SB;
        LRT2(h28_3_0, hp3, 8192); SB;
        MF(acc[1][1][0], W12, "a", cat2(h32_1_0, h32_1_1), "v"); SB;
        LRT2(h28_3_1, hp3, 8704); SB;
        GLD4(V23, "a", vo16, vsrc + 20480, 3072, " sc1"); SB;
        __builtin_amdgcn_s_setprio(0); SB;
        asm volatile("s_waitcnt lgkmcnt(0)" ::: "memory"); SB;
        MF(acc[1][2][0], W8, "a", cat2(h28_2_0, h28_2_1), "v"); SB;
        LRT2(h33_0_0, hp0, 13312); SB;
        LRT2(h33_0_1, hp0, 13824); SB;
        MF(acc[1][3][0], W8, "a", cat2(h28_3_0, h28_3_1), "v"); SB;
        LRT2(h33_1_0, hp1, 13312); SB;
        LRT2(h33_1_1, hp1, 13824); SB;
        __builtin_amdgcn_s_setprio(0); SB;
        asm volatile("s_waitcnt lgkmcnt(0)" ::: "memory"); SB;
        MF(acc[1][0][0], W13, "a", cat2(h33_0_0, h33_0_1), "v"); SB;
        LRT2(h29_2_0, hp2, 9216); SB;
        MF(ssum[1], ones4, "v", cat2(h33_0_0, h33_0_1), "v"); SB;
        LRT2(h29_2_1, hp2, 9728); SB;
        MF(ssq[1], cat2(h33_0_0, h33_0_1), "v", cat2(h33_0_0, h33_0_1), "v"); SB;
        LRT2(h29_3_0, hp3, 9216); SB;
        MF(acc[1][1][0], W13, "a", cat2(h33_1_0, h33_1_1), "v"); SB;
        LRT2(h29_3_1, hp3, 9728); SB;
        GLD4(V24, "a", vo16, vsrc + 24576, 0, " sc1"); SB;
        __builtin_amdgcn_s_setprio(0); SB;
        asm volatile("s_waitcnt lgkmcnt(0)" ::: "memory"); SB;
        MF(acc[1][2][0], W9, "a", cat2(h29_2_0, h29_2_1), "v"); SB;
        LRT2(h34_0_0, hp0, 14336); SB;
        LRT2(h34_0_1, hp0, 14848); SB;
        MF(acc[1][3][0], W9, "a", cat2(h29_3_0, h29_3_1), "v"); SB;
        LRT2(h34_1_0, hp1, 14336); SB;
        LRT2(h34_1_1, hp1, 14848); SB;
        __builtin_amdgcn_s_setprio(0); SB;
        asm volatile("s_waitcnt lgkmcnt(0)" ::: "memory"); SB;
        MF(acc[1][0][0], W14, "a", cat2(h34_0_0, h34_0_1), "v"); SB;
        LRT2(h30_2_0, hp2, 10240); SB;
        MF(ssum[1], ones4, "v", cat2(h34_0_0, h34_0_1), "v"); SB;
        LRT2(h30_2_1, hp2, 10752); SB;
        MF(ssq[1], cat2(h34_0_0, h34_0_1), "v", cat2(h34_0_0, h34_0_1), "v"); SB;
        LRT2(h30_3_0, hp3, 10240); SB;
        MF(acc[1][1][0], W14, "a", cat2(h34_1_0, h34_1_1), "v"); SB;
        LRT2(h30_3_1, hp3, 10752); SB;
        GLD4(V25, "a", vo16, vsrc + 24576, 1024, " sc1"); SB;
        __builtin_amdgcn_s_setprio(0); SB;
        asm volatile("s_waitcnt lgkmcnt(0)" ::: "memory"); SB;
        MF(acc[1][2][0], W10, "a", cat2(h30_2_0, h30_2_1), "v"); SB;
        LRT2(h35_0_0, hp0, 15360); SB;
        LRT2(h35_0_1, hp0, 15872); SB;
        MF(acc[1][3][0], W10, "a", cat2(h30_3_0, h30_3_1), "v"); SB;
        LRT2(h35_1_0, hp1, 15360); SB;
        LRT2(h35_1_1, hp1, 15872); SB;
        asm volatile("s_waitcnt vmcnt(4)" ::: "memory"); SB;
        BAR(); SB;
        __builtin_amdgcn_s_setprio(3); SB;
        asm volatile("s_waitcnt lgkmcnt(0)" ::: "memory"); SB;
        MF(acc[1][0][0], W15, "a", cat2(h35_0_0, h35_0_1), "v"); SB;
        LRT2(h31_2_0, hp2, 11264); SB;
        MF(ssum[1], ones4, "v", cat2(h35_0_0, h35_0_1), "v"); SB;
        LRT2(h31_2_1, hp2, 11776); SB;
        MF(ssq[1], cat2(h35_0_0, h35_0_1), "v", cat2(h35_0_0, h35_0_1), "v"); SB;
        LRT2(h31_3_0, hp3, 11264); SB;
        MF(acc[1][1][0], W15, "a", cat2(h35_1_0, h35_1_1), "v"); SB;
        LRT2(h31_3_1, hp3, 11776); SB;
        GLD4(V26, "a", vo16, vsrc + 24576, 2048, " sc1"); SB;
        __builtin_amdgcn_s_setprio(2); SB;
        asm volatile("s_waitcnt lgkmcnt(0)" ::: "memory"); SB;
        MF(acc[1][2][0], W11, "a", cat2(h31_2_0, h31_2_1), "v"); SB;
        LRT2(h36_0_0, hp0, 16384); SB;
        LRT2(h36_0_1, hp0, 16896); SB;
        MF(acc[1][3][0], W11, "a", cat2(h31_3_0, h31_3_1), "v"); SB;
        LRT2(h36_1_0, hp1, 16384); SB;
        LRT2(h36_1_1, hp1, 16896); SB;
        __builtin_amdgcn_s_setprio(1); SB;
        asm volatile("s_waitcnt lgkmcnt(0)" ::: "memory"); SB;
        MF(acc[1][0][0], W16, "a", cat2(h36_0_0, h36_0_1), "v"); SB;
        LRT2(h32_2_0, hp2, 12288); SB;
        MF(ssum[1], ones4, "v", cat2(h36_0_0, h36_0_1), "v"); SB;
        LRT2(h32_2_1, hp2, 12800); SB;
        GLD1(fl2, voF1, flH, 0, " sc1"); SB;
        MF(ssq[1], cat2(h36_0_0, h36_0_1), "v", cat2(h36_0_0, h36_0_1), "v"); SB;
        LRT2(h32_3_0, hp3, 12288); SB;
        MF(acc[1][1][0], W16, "a", cat2(h36_1_0, h36_1_1), "v"); SB;
        LRT2(h32_3_1, hp3, 12800); SB;
        __builtin_amdgcn_s_setprio(0); SB;
        asm volatile("s_waitcnt lgkmcnt(0)" ::: "memory"); SB;
        MF(acc[1][2][0], W12, "a", cat2(h32_2_0, h32_2_1), "v"); SB;
        LRT2(h37_0_0, hp0, 17408); SB;
        LRT2(h37_0_1, hp0, 17920); SB;
        GLD4(V27, "a", vo16, vsrc + 24576, 3072, " sc1"); SB;
        MF(acc[1][3][0], W12, "a", cat2(h32_3_0, h32_3_1), "v"); SB;
        LRT2(h37_1_0, hp1, 17408); SB;
        LRT2(h37_1_1, hp1, 17920); SB;
        __builtin_amdgcn_s_setprio(0); SB;
        asm volatile("s_waitcnt lgkmcnt(0)" ::: "memory"); SB;
        MF(acc[1][0][0], W17, "a", cat2(h37_0_0, h37_0_1), "v"); SB;
        LRT2(h33_2_0, hp2, 13312); SB;
        MF(ssum[1], ones4, "v", cat2(h37_0_0, h37_0_1), "v"); SB;
        LRT2(h33_2_1, hp2, 13824); SB;
        MF(ssq[1], cat2(h37_0_0, h37_0_1), "v", cat2(h37_0_0, h37_0_1), "v"); SB;
        LRT2(h33_3_0, hp3, 13312); SB;
        MF(acc[1][1][0], W17, "a", cat2(h37_1_0, h37_1_1), "v"); SB;
        LRT2(h33_3_1, hp3, 13824); SB;
        GLD4(V28, "a", vo16, vsrc + 28672, 0, " sc1"); SB;
        __builtin_amdgcn_s_setprio(0); SB;
        asm volatile("s_waitcnt lgkmcnt(0)" ::: "memory"); SB;
        MF(acc[1][2][0], W13, "a", cat2(h33_2_0, h33_2_1), "v"); SB;
        LRT2(h38_0_0, hp0, 18432); SB;
        LRT2(h38_0_1, hp0, 18944); SB;
        MF(acc[1][3][0], W13, "a", cat2(h33_3_0, h33_3_1), "v"); SB;
        LRT2(h38_1_0, hp1, 18432); SB;
        LRT2(h38_1_1, hp1, 18944); SB;
        __builtin_amdgcn_s_setprio(0); SB;
        asm volatile("s_waitcnt lgkmcnt(0)" ::: "memory"); SB;
        MF(acc[1][0][0], W18, "a", cat2(h38_0_0, h38_0_1), "v"); SB;
        LRT2(h34_2_0, hp2, 14336); SB;
        MF(ssum[1], ones4, "v", cat2(h38_0_0, h38_0_1), "v"); SB;
        LRT2(h34_2_1, hp2, 14848); SB;
        MF(ssq[1], cat2(h38_0_0, h38_0_1), "v", cat2(h38_0_0, h38_0_1), "v"); SB;
        LRT2(h34_3_0, hp3, 14336); SB;
        MF(acc[1][1][0], W18, "a", cat2(h38_1_0, h38_1_1), "v"); SB;
        LRT2(h34_3_1, hp3, 14848); SB;
        GLD4(V29, "a", vo16, vsrc + 28672, 1024, " sc1"); SB;
        __builtin_amdgcn_s_setprio(0); SB;
        asm volatile("s_waitcnt lgkmcnt(0)" ::: "memory"); SB;
        MF(acc[1][2][0], W14, "a", cat2(h34_2_0, h34_2_1), "v"); SB;
        LRT2(h39_0_0, hp0, 19456); SB;
        LRT2(h39_0_1, hp0, 19968); SB;
        MF(acc[1][3][0], W14, "a", cat2(h34_3_0, h34_3_1), "v"); SB;
        LRT2(h39_1_0, hp1, 19456); SB;
        LRT2(h39_1_1, hp1, 19968); SB;
        __builtin_amdgcn_s_setprio(0); SB;
        asm volatile("s_waitcnt lgkmcnt(0)" ::: "memory"); SB;
        MF(acc[1][0][0], W19, "a", cat2(h39_0_0, h39_0_1), "v"); SB;
        LRT2(h35_2_0, hp2, 15360); SB;
        MF(ssum[1], ones4, "v", cat2(h39_0_0, h39_0_1), "v"); SB;
        LRT2(h35_2_1, hp2, 15872); SB;
        MF(ssq[1], cat2(h39_0_0, h39_0_1), "v", cat2(h39_0_0, h39_0_1), "v"); SB;
        LRT2(h35_3_0, hp3, 15360); SB;
        MF(acc[1][1][0], W19, "a", cat2(h39_1_0, h39_1_1), "v"); SB;
        LRT2(h35_3_1, hp3, 15872); SB;
        asm volatile("s_waitcnt vmcnt(3)" : "+v"(fl2) :: "memory"); SB;
        CHECK1(fl2, flH, voF1); SB;
        __builtin_amdgcn_s_setprio(0); SB;
        asm volatile("s_waitcnt lgkmcnt(0)" ::: "memory"); SB;
        MF(acc[1][2][0], W15, "a", cat2(h35_2_0, h35_2_1), "v"); SB;
        LRT2(h36_2_0, hp2, 16384); SB;
        LRT2(h36_2_1, hp2, 16896); SB;
        MFNOP(ssq[1]); SB;
        {   float q = (a & 2) ? ((a & 1) ? ssq[1][3] : ssq[1][2]) : ((a & 1) ? ssq[1][1] : ssq[1][0]);
            f32x2 o; o[0] = ssum[1][0] * (-1.f / D_); o[1] = __builtin_amdgcn_rcpf(fmaxf(q * (1.f / D_) - o[0] * o[0], 0.f) + 1e-5f);
            if (g == (a >> 2)) LWR2(sp0, o, 512); } SB;
        MF(acc[1][3][0], W15, "a", cat2(h35_3_0, h35_3_1), "v"); SB;
        LRT2(h36_3_0, hp3, 16384); SB;
        LRT2(h36_3_1, hp3, 16896); SB;
        __builtin_amdgcn_s_setprio(0); SB;
        asm volatile("s_waitcnt lgkmcnt(0)" ::: "memory"); SB;
        MF(acc[1][2][0], W16, "a", cat2(h36_2_0, h36_2_1), "v"); SB;
        LRT2(h37_2_0, hp2, 17408); SB;
        LRT2(h37_2_1, hp2, 17920); SB;
        GLD4(HA0_0, "v", vo16, fsA + 0, 0, " sc1"); SB;
        MF(acc[1][3][0], W16, "a", cat2(h36_3_0, h36_3_1), "v"); SB;
        LRT2(h37_3_0, hp3, 17408); SB;
        LRT2(h37_3_1, hp3, 17920); SB;
        GLD4(HA0_1, "v", vo16, fsA + 49152, 0, " sc1"); SB;
        GLD4(HA1_0, "v", vo16, fsA + 0, 1024, " sc1"); SB;
        BAR(); SB;
        __builtin_amdgcn_s_setprio(3); SB;
        asm volatile("s_waitcnt lgkmcnt(0)" ::: "memory"); SB;
        MF(acc[1][2][0], W17, "a", cat2(h37_2_0, h37_2_1), "v"); SB;
        LRT2(h38_2_0, hp2, 18432); SB;
        LRT2(h38_2_1, hp2, 18944); SB;
        GLD4(HA1_1, "v", vo16, fsA + 49152, 1024, " sc1"); SB;
        f32x2 st1_0, st1_1, st1_2, st1_3;
        LRD2(st1_0, sp0, 512); SB;
        LRD2(st1_1, sp1, 512); SB;
        LRD2(st1_2, sp2, 512); SB;
        LRD2(st1_3, sp3, 512); SB;
        MF(acc[1][3][0], W17, "a", cat2(h37_3_0, h37_3_1), "v"); SB;
        LRT2(h38_3_0, hp3, 18432); SB;
        LRT2(h38_3_1, hp3, 18944); SB;
        GLD4(HA2_0, "v", vo16, fsA + 0, 2048, " sc1"); SB;
        GLD4(HA2_1, "v", vo16, fsA + 49152, 2048, " sc1"); SB;
        __builtin_amdgcn_s_setprio(2); SB;
        asm volatile("s_waitcnt lgkmcnt(0)" ::: "memory"); SB;
        MF(acc[1][2][0], W18, "a", cat2(h38_2_0, h38_2_1), "v"); SB;
        LRT2(h39_2_0, hp2, 19456); SB;
        LRT2(h39_2_1, hp2, 19968); SB;
        GLD4(HA3_0, "v", vo16, fsA + 0, 3072, " sc1"); SB;
        float z1_0_0a = acc[1][0][0][0], z1_0_0b = acc[1][0][0][1], z1_0_0c = acc[1][0][0][2], z1_0_0d = acc[1][0][0][3];
        EPI_A(z1_0_0a, z1_0_0c, st1_0[0], csv[0][0], csv[0][2]); EPI_A(z1_0_0b, z1_0_0d, st1_0[0], csv[0][1], csv[0][3]); SB;
        MF(acc[1][3][0], W18, "a", cat2(h38_3_0, h38_3_1), "v"); SB;
        LRT2(h39_3_0, hp3, 19456); SB;
        LRT2(h39_3_1, hp3, 19968); SB;
        GLD4(HA3_1, "v", vo16, fsA + 49152, 3072, " sc1"); SB;
        GLD4(HA4_0, "v", vo16, fsA + 4096, 0, " sc1"); SB;
        EPI_B(z1_0_0a, z1_0_0c, st1_0[1]); EPI_B(z1_0_0b, z1_0_0d, st1_0[1]); SB;
        __builtin_amdgcn_s_setprio(1); SB;
        asm volatile("s_waitcnt lgkmcnt(0)" ::: "memory"); SB;
        MF(acc[1][2][0], W19, "a", cat2(h39_2_0, h39_2_1), "v"); SB;
        GLD4(HA4_1, "v", vo16, fsA + 53248, 0, " sc1"); SB;
        EPI_C(z1_0_0a, z1_0_0b, z1_0_0c, z1_0_0d); SB;
        EPI_D(z1_0_0a, z1_0_0b, xp0, 512); SB;
        MF(acc[1][3][0], W19, "a", cat2(h39_3_0, h39_3_1), "v"); SB;
        GLD4(HA5_0, "v", vo16, fsA + 4096, 1024, " sc1"); SB;
        GLD4(HA5_1, "v", vo16, fsA + 53248, 1024, " sc1"); SB;
        float z1_1_0a = acc[1][1][0][0], z1_1_0b = acc[1][1][0][1], z1_1_0c = acc[1][1][0][2], z1_1_0d = acc[1][1][0][3];
        EPI_A(z1_1_0a, z1_1_0c, st1_1[0], csv[0][0], csv[0][2]); EPI_A(z1_1_0b, z1_1_0d, st1_1[0], csv[0][1], csv[0][3]); SB;
        EPI_B(z1_1_0a, z1_1_0c, st1_1[1]); EPI_B(z1_1_0b, z1_1_0d, st1_1[1]); SB;
        EPI_C(z1_1_0a, z1_1_0b, z1_1_0c, z1_1_0d); SB;
        EPI_D(z1_1_0a, z1_1_0b, xp1, 512); SB;
        STAMP(10);
        MFNOP(acc[1][3][0]); SB;
        asm volatile("s_waitcnt vmcnt(10)" ::: "memory"); SB;
        __builtin_amdgcn_s_setprio(0); SB;
        MF0(oc[0][0][0], V0, "a", HA0_0, "v"); SB;
        float z1_2_0a = acc[1][2][0][0], z1_2_0b = acc[1][2][0][1], z1_2_0c = acc[1][2][0][2], z1_2_0d = acc[1][2][0][3];
        EPI_A(z1_2_0a, z1_2_0c, st1_2[0], csv[0][0], csv[0][2]); EPI_A(z1_2_0b, z1_2_0d, st1_2[0], csv[0][1], csv[0][3]); SB;
        MF0(oc[0][0][1], V1, "a", HA0_0, "v"); SB;
        EPI_B(z1_2_0a, z1_2_0c, st1_2[1]); EPI_B(z1_2_0b, z1_2_0d, st1_2[1]); SB;
        MF0(oc[0][0][2], V2, "a", HA0_0, "v"); SB;
        EPI_C(z1_2_0a, z1_2_0b, z1_2_0c, z1_2_0d); SB;
        EPI_D(z1_2_0a, z1_2_0b, xp2, 512); SB;
        MF0(oc[0][0][3], V3, "a", HA0_0, "v"); SB;
        float z1_3_0a = acc[1][3][0][0], z1_3_0b = acc[1][3][0][1], z1_3_0c = acc[1][3][0][2], z1_3_0d = acc[1][3][0][3];
        EPI_A(z1_3_0a, z1_3_0c, st1_3[0], csv[0][0], csv[0][2]); EPI_A(z1_3_0b, z1_3_0d, st1_3[0], csv[0][1], csv[0][3]); SB;
        MF0(oc[0][0][4], V4, "a", HA0_0, "v"); SB;
        EPI_B(z1_3_0a, z1_3_0c, st1_3[1]); EPI_B(z1_3_0b, z1_3_0d, st1_3[1]); SB;
        MF0(oc[0][1][0], V0, "a", HA0_1, "v"); SB;
        EPI_C(z1_3_0a, z1_3_0b, z1_3_0c, z1_3_0d); SB;
        EPI_D(z1_3_0a, z1_3_0b, xp3, 512); SB;
        MF0(oc[0][1][1], V1, "a", HA0_1, "v"); SB;
        MF0(oc[0][1][2], V2, "a", HA0_1, "v"); SB;
        MF0(oc[0][1][3], V3, "a", HA0_1, "v"); SB;
        MF0(oc[0][1][4], V4, "a", HA0_1, "v"); SB;
        asm volatile("s_waitcnt vmcnt(8)" ::: "memory"); SB;
        __builtin_amdgcn_s_setprio(0); SB;
        MF(oc[0][0][0], V5, "a", HA1_0, "v"); SB;
        MF(oc[0][0][1], V6, "a", HA1_0, "v"); SB;
        MF(oc[0][0][2], V7, "a", HA1_0, "v"); SB;
        MF(oc[0][0][3], V8, "a", HA1_0, "v"); SB;
        MF(oc[0][0][4], V9, "a", HA1_0, "v"); SB;
        MF(oc[0][1][0], V5, "a", HA1_1, "v"); SB;
        MF(oc[0][1][1], V6, "a", HA1_1, "v"); SB;
        MF(oc[0][1][2], V7, "a", HA1_1, "v"); SB;
        MF(oc[0][1][3], V8, "a", HA1_1, "v"); SB;
        MF(oc[0][1][4], V9, "a", HA1_1, "v"); SB;
        asm volatile("s_waitcnt lgkmcnt(0)" ::: "memory"); SB;
        BAR(); SB;
        asm volatile("s_waitcnt vmcnt(6)" ::: "memory"); SB;
        __builtin_amdgcn_s_setprio(3); SB;
        MF(oc[0][0][0], V10, "a", HA2_0, "v"); SB;
        u32x2 ho1_0, ho1_1, ho1_2, ho1_3, ho1_4, ho1_5;
        LRD2S(ho1_0, xr, 0, 1); SB;
        LRD2S(ho1_1, xr, 2, 3); SB;
        LRD2S(ho1_2, xr, 4, 5); SB;
        LRD2S(ho1_3, xr, 6, 7); SB;
        LRD2S(ho1_4, xr, 8, 9); SB;
        LRD2S(ho1_5, xr, 10, 11); SB;
        MF(oc[0][0][1], V11, "a", HA2_0, "v"); SB;
        MF(oc[0][0][2], V12, "v", HA2_0, "v"); SB;
        MF(oc[0][0][3], V13, "v", HA2_0, "v"); SB;
        asm volatile("s_waitcnt lgkmcnt(0)" ::: "memory"); SB;
        GST4(vo16, cat2(ho1_0, ho1_1), hoA + 3145728, 0); SB;
        GST4(vo16, cat2(ho1_2, ho1_3), hoA + 3145728, 1024); SB;
        GST4(vo16, cat2(ho1_4, ho1_5), hoA + 3145728, 2048); SB;
        MF(oc[0][0][4], V14, "v", HA2_0, "v"); SB;
        MF(oc[0][1][0], V10, "a", HA2_1, "v"); SB;
        MF(oc[0][1][1], V11, "a", HA2_1, "v"); SB;
        MF(oc[0][1][2], V12, "v", HA2_1, "v"); SB;
        MF(oc[0][1][3], V13, "v", HA2_1, "v"); SB;
        MF(oc[0][1][4], V14, "v", HA2_1, "v"); SB;
        asm volatile("s_waitcnt vmcnt(7)" ::: "memory"); SB;
        __builtin_amdgcn_s_setprio(2); SB;
        MF(oc[0][0][0], V15, "v", HA3_0, "v"); SB;
        MF(oc[0][0][1], V16, "a", HA3_0, "v"); SB;
        MF(oc[0][0][2], V17, "a", HA3_0, "v"); SB;
        MF(oc[0][0][3], V18, "a", HA3_0, "v"); SB;
        MF(oc[0][0][4], V19, "a", HA3_0, "v"); SB;
        MF(oc[0][1][0], V15, "v", HA3_1, "v"); SB;
        MF(oc[0][1][1], V16, "a", HA3_1, "v"); SB;
        MF(oc[0][1][2], V17, "a", HA3_1, "v"); SB;
        MF(oc[0][1][3], V18, "a", HA3_1, "v"); SB;
        MF(oc[0][1][4], V19, "a", HA3_1, "v"); SB;
        asm volatile("s_waitcnt vmcnt(5)" ::: "memory"); SB;
        __builtin_amdgcn_s_setprio(1); SB;
        MF(oc[0][0][0], V20, "a", HA4_0, "v"); SB;
        MF(oc[0][0][1], V21, "a", HA4_0, "v"); SB;
        MF(oc[0][0][2], V22, "a", HA4_0, "v"); SB;
        MF(oc[0][0][3], V23, "a", HA4_0, "v"); SB;
        MF(oc[0][0][4], V24, "a", HA4_0, "v"); SB;
        MF(oc[0][1][0], V20, "a", HA4_1, "v"); SB;
        MF(oc[0][1][1], V21, "a", HA4_1, "v"); SB;
        MF(oc[0][1][2], V22, "a", HA4_1, "v"); SB;
        MF(oc[0][1][3], V23, "a", HA4_1, "v"); SB;
        MF(oc[0][1][4], V24, "a", HA4_1, "v"); SB;
        asm volatile("s_waitcnt vmcnt(3)" ::: "memory"); SB;
        __builtin_amdgcn_s_setprio(0); SB;
        MF(oc[0][0][0], V25, "a", HA5_0, "v"); SB;
        MF(oc[0][0][1], V26, "a", HA5_0, "v"); SB;
        MF(oc[0][0][2], V27, "a", HA5_0, "v"); SB;
        MF(oc[0][0][3], V28, "a", HA5_0, "v"); SB;
        MF(oc[0][0][4], V29, "a", HA5_0, "v"); SB;
        MF(oc[0][1][0], V25, "a", HA5_1, "v"); SB;
        MF(oc[0][1][1], V26, "a", HA5_1, "v"); SB;
        MF(oc[0][1][2], V27, "a", HA5_1, "v"); SB;
        MF(oc[0][1][3], V28, "a", HA5_1, "v"); SB;
        MF(oc[0][1][4], V29, "a", HA5_1, "v"); SB;
        STAMP(12);
        MFNOP(oc[0][1][4]); SB;
        GLD4(HB[0], "v", vo16, fsB + 0, 0, " sc1"); SB;
        LWR4(rpw, oc[0][0][0], 0); SB;
        GLD4(HB[1], "v", vo16, fsB + 49152, 0, " sc1"); SB;
        LWR4(rpw, oc[0][0][1], 1024); SB;
        GLD4(HB[2], "v", vo16, fsB + 0, 1024, " sc1"); SB;
        LWR4(rpw, oc[0][0][2], 2048); SB;
        GLD4(HB[3], "v", vo16, fsB + 49152, 1024, " sc1"); SB;
        LWR4(rpw, oc[0][0][3], 3072); SB;
        GLD4(HB[4], "v", vo16, fsB + 0, 2048, " sc1"); SB;
        LWR4(rpw, oc[0][0][4], 4096); SB;
        GLD4(HB[5], "v", vo16, fsB + 49152, 2048, " sc1"); SB;
        LWR4(rpw, oc[0][1][0], 5120); SB;
        GLD4(HB[6], "v", vo16, fsB + 0, 3072, " sc1"); SB;
        LWR4(rpw, oc[0][1][1], 6144); SB;
        GLD4(HB[7], "v", vo16, fsB + 49152, 3072, " sc1"); SB;
        LWR4(rpw, oc[0][1][2], 7168); SB;
        GLD4(HB[8], "v", vo16, fsB + 4096, 0, " sc1"); SB;
        LWR4(rpw, oc[0][1][3], 8192); SB;
        GLD4(HB[9], "v", vo16, fsB + 53248, 0, " sc1"); SB;
        LWR4(rpw, oc[0][1][4], 9216); SB;
        GLD4(HB[10], "v", vo16, fsB + 4096, 1024, " sc1"); SB;
        GLD4(HB[11], "v", vo16, fsB + 53248, 1024, " sc1"); SB;
        asm volatile("s_waitcnt lgkmcnt(0)" ::: "memory"); SB;
        BAR(); SB;
        FINISH(0); SB;
        asm volatile("s_waitcnt vmcnt(1)" : "+v"(HB[0]), "+v"(HB[1]), "+v"(HB[2]), "+v"(HB[3]), "+v"(HB[4]), "+v"(HB[5]), "+v"(HB[6]), "+v"(HB[7]), "+v"(HB[8]), "+v"(HB[9]), "+v"(HB[10]), "+v"(HB[11]) :: "memory"); SB;
        HB_CHECK(); SB;
        STAMP(13);
        __builtin_amdgcn_s_setprio(3); SB;
        MF0(oc[1][0][0], V0, "a", HB[0], "v"); SB;
        MF0(oc[1][0][1], V1, "a", HB[0], "v"); SB;
        MF0(oc[1][0][2], V2, "a", HB[0], "v"); SB;
        MF0(oc[1][0][3], V3, "a", HB[0], "v"); SB;
        MF0(oc[1][0][4], V4, "a", HB[0], "v"); SB;
        MF0(oc[1][1][0], V0, "a", HB[1], "v"); SB;
        MF0(oc[1][1][1], V1, "a", HB[1], "v"); SB;
        MF0(oc[1][1][2], V2, "a", HB[1], "v"); SB;
        MF0(oc[1][1][3], V3, "a", HB[1], "v"); SB;
        MF0(oc[1][1][4], V4, "a", HB[1], "v"); SB;
        __builtin_amdgcn_s_setprio(2); SB;
        MF(oc[1][0][0], V5, "a", HB[2], "v"); SB;
        MF(oc[1][0][1], V6, "a", HB[2], "v"); SB;
        MF(oc[1][0][2], V7, "a", HB[2], "v"); SB;
        MF(oc[1][0][3], V8, "a", HB[2], "v"); SB;
        MF(oc[1][0][4], V9, "a", HB[2], "v"); SB;
        MF(oc[1][1][0], V5, "a", HB[3], "v"); SB;
        MF(oc[1][1][1], V6, "a", HB[3], "v"); SB;
        MF(oc[1][1][2], V7, "a", HB[3], "v"); SB;
        MF(oc[1][1][3], V8, "a", HB[3], "v"); SB;
        MF(oc[1][1][4], V9, "a", HB[3], "v"); SB;
        __builtin_amdgcn_s_setprio(1); SB;
        MF(oc[1][0][0], V10, "a", HB[4], "v"); SB;
        MF(oc[1][0][1], V11, "a", HB[4], "v"); SB;
        MF(oc[1][0][2], V12, "v", HB[4], "v"); SB;
        MF(oc[1][0][3], V13, "v", HB[4], "v"); SB;
        MF(oc[1][0][4], V14, "v", HB[4], "v"); SB;
        MF(oc[1][1][0], V10, "a", HB[5], "v"); SB;
        MF(oc[1][1][1], V11, "a", HB[5], "v"); SB;
        MF(oc[1][1][2], V12, "v", HB[5], "v"); SB;
        MF(oc[1][1][3], V13, "v", HB[5], "v"); SB;
        MF(oc[1][1][4], V14, "v", HB[5], "v"); SB;
        __builtin_amdgcn_s_setprio(0); SB;
        MF(oc[1][0][0], V15, "v", HB[6], "v"); SB;
        MF(oc[1][0][1], V16, "a", HB[6], "v"); SB;
        MF(oc[1][0][2], V17, "a", HB[6], "v"); SB;
        MF(oc[1][0][3], V18, "a", HB[6], "v"); SB;
        MF(oc[1][0][4], V19, "a", HB[6], "v"); SB;
        MF(oc[1][1][0], V15, "v", HB[7], "v"); SB;
        MF(oc[1][1][1], V16, "a", HB[7], "v"); SB;
        MF(oc[1][1][2], V17, "a", HB[7], "v"); SB;
        MF(oc[1][1][3], V18, "a", HB[7], "v"); SB;
        MF(oc[1][1][4], V19, "a", HB[7], "v"); SB;
        asm volatile("s_waitcnt lgkmcnt(0)" ::: "memory"); BAR(); SB;
        __builtin_amdgcn_s_setprio(3); SB;
        MF(oc[1][0][0], V20, "a", HB[8], "v"); SB;
        MF(oc[1][0][1], V21, "a", HB[8], "v"); SB;
        MF(oc[1][0][2], V22, "a", HB[8], "v"); SB;
        MF(oc[1][0][3], V23, "a", HB[8], "v"); SB;
        MF(oc[1][0][4], V24, "a", HB[8], "v"); SB;
        MF(oc[1][1][0], V20, "a", HB[9], "v"); SB;
        MF(oc[1][1][1], V21, "a", HB[9], "v"); SB;
        MF(oc[1][1][2], V22, "a", HB[9], "v"); SB;
        MF(oc[1][1][3], V23, "a", HB[9], "v"); SB;
        MF(oc[1][1][4], V24, "a", HB[9], "v"); SB;
        __builtin_amdgcn_s_setprio(2); SB;
        MF(oc[1][0][0], V25, "a", HB[10], "v"); SB;
        MF(oc[1][0][1], V26, "a", HB[10], "v"); SB;
        MF(oc[1][0][2], V27, "a", HB[10], "v"); SB;
        MF(oc[1][0][3], V28, "a", HB[10], "v"); SB;
        MF(oc[1][0][4], V29, "a", HB[10], "v"); SB;
        MF(oc[1][1][0], V25, "a", HB[11], "v"); SB;
        MF(oc[1][1][1], V26, "a", HB[11], "v"); SB;
        MF(oc[1][1][2], V27, "a", HB[11], "v"); SB;
        MF(oc[1][1][3], V28, "a", HB[11], "v"); SB;
        MF(oc[1][1][4], V29, "a", HB[11], "v"); SB;
        MFNOP(oc[1][1][4]); SB;
        LWR4(rpw, oc[1][0][0], 0); SB;
        LWR4(rpw, oc[1][0][1], 1024); SB;
        LWR4(rpw, oc[1][0][2], 2048); SB;
        LWR4(rpw, oc[1][0][3], 3072); SB;
        LWR4(rpw, oc[1][0][4], 4096); SB;
        LWR4(rpw, oc[1][1][0], 5120); SB;
        LWR4(rpw, oc[1][1][1], 6144); SB;
        LWR4(rpw, oc[1][1][2], 7168); SB;
        LWR4(rpw, oc[1][1][3], 8192); SB;
        LWR4(rpw, oc[1][1][4], 9216); SB;
        asm volatile("s_waitcnt lgkmcnt(0)" ::: "memory"); SB;
        BAR(); SB;
        FINISH(1); SB;

    }
    FINE_END();
    STAMP_END(1);
    asm volatile(".Lkm_end:");
}

extern "C" void kernel_launch(void* const* d_in, const int* in_sizes, int n_in,
                              void* d_out, int out_size, void* d_ws, size_t ws_size,
                              hipStream_t stream) {
    const u16* q = (const u16*)d_in[0];
    const u16* k = (const u16*)d_in[1];
    const u16* v = (const u16*)d_in[2];
    const u16* sk = (const u16*)d_in[3];
    const u16* hk = (const u16*)d_in[4];
    const u16* Dv = (const u16*)d_in[5];
    const u16* wg = (const u16*)d_in[7];
    const u16* wu = (const u16*)d_in[8];
    const u16* wd = (const u16*)d_in[9];
    char* ws = (char*)d_ws;
    (void)hipFuncSetAttribute((const void*)k_main, hipFuncAttributeMaxDynamicSharedMemorySize, MAIN_LDS);
    k1_prep<<<K1_TR + K1_GU + K1_LN, 256, 0, stream>>>(q, k, v, sk, wg, wu, ws);
    k_main<<<256, 512, MAIN_LDS, stream>>>(hk, Dv, wd, ws, (u16*)d_out);
}
```
